```python
import torch

def _softmax(x, axis=-1):
    return torch.softmax(x, dim=axis)
def _gelu(x):
    return torch.nn.functional.gelu(x, approximate="tanh")
def _silu(x):
    return torch.nn.functional.silu(x)
def _sigmoid(x):
    return torch.sigmoid(x)
def _layernorm(x, axis=-1, eps=1e-5):
    m = x.mean(dim=axis, keepdim=True)
    v = x.var(dim=axis, keepdim=True, unbiased=False)
    return (x - m) / torch.sqrt(v + eps)
def _scatter_add(tgt, idx, src, axis=0):
    return tgt.clone().index_add_(axis, idx, src)
def _segment_sum(src, seg, n):
    out = torch.zeros((n,) + src.shape[1:], dtype=src.dtype)
    return out.index_add_(0, seg, src)
def _pad(x, pw):
    flat = tuple(v for pair in reversed(pw) for v in pair)
    return torch.nn.functional.pad(x, flat)

kernel_name = 'standalone__gqa_softmax__sampling'

def setup_inputs():

    _w = (lambda a: (lambda t: t.to(torch.bfloat16) if t.is_floating_point() else t)(torch.as_tensor(a)))
    B, Hq, Hkv, Sq, Sk, D = 8, 32, 8, 32, 2048, 128
    return {
        "q": _w((lambda *s: torch.randn(*s, dtype=torch.bfloat16) * 0.8)(B, Hq, Sq, D)),
        "k": _w((lambda *s: torch.randn(*s, dtype=torch.bfloat16) * 0.8)(B, Hkv, Sk, D)),
        "v": _w((lambda *s: torch.randn(*s, dtype=torch.bfloat16) * 0.05)(B, Hkv, Sk, D)),
    }

def reference(q, k, v):
    B, Hq, Sq, D = q.shape
    Hkv = k.shape[1]
    rep = Hq // Hkv
    k = (lambda x, n, axis: torch.repeat_interleave(x, n, dim=axis))(k, rep, axis=1)
    v = (lambda x, n, axis: torch.repeat_interleave(x, n, dim=axis))(v, rep, axis=1)
    scale = D ** -0.5
    scores = torch.einsum("bhqd,bhkd->bhqk", q, k) * scale
    p = _softmax(scores, axis=-1)
    return torch.einsum("bhqk,bhkd->bhqd", p, v)
```

```cpp
#include <hip/hip_runtime.h>
#include <hip/hip_bf16.h>
#include <stdint.h>

typedef __attribute__((ext_vector_type(8))) short bf16x8;
typedef __attribute__((ext_vector_type(4))) short s16x4;
typedef __attribute__((ext_vector_type(16))) float f32x16;
typedef __attribute__((ext_vector_type(4))) float f32x4;
typedef __attribute__((ext_vector_type(2))) float f32x2;
typedef __attribute__((ext_vector_type(2))) __bf16 bf16x2_t;
typedef __attribute__((ext_vector_type(4))) unsigned u32x4;
typedef __attribute__((ext_vector_type(2))) unsigned u32x2;

constexpr int Bn = 8, HQ = 32, HKV = 8, SQ = 32, SK = 2048, D = 128;
constexpr int NW = 8;
constexpr int TK = 32;
constexpr int NT = SK / TK / NW;
constexpr float CEXP = 0.088388347648318447f * 1.4426950408889634f;
constexpr int SLOT = TK * D * 2;
constexpr int LDS_STATS = NW * 2 * SLOT;
constexpr int LDS_BYTES = LDS_STATS + NW * 32 * 2 * 4;

#define LDS_AS __attribute__((address_space(3)))
#define GLB_AS __attribute__((address_space(1)))

__device__ __forceinline__ unsigned pk2(float a, float b) {
  f32x2 v = {a, b};
  bf16x2_t r = __builtin_convertvector(v, bf16x2_t);
  return __builtin_bit_cast(unsigned, r);
}

__global__ __launch_bounds__(64 * NW, 2) void gqa_attn_kernel(const __hip_bfloat16* __restrict__ Qp, const __hip_bfloat16* __restrict__ Kp,
                                                              const __hip_bfloat16* __restrict__ Vp, __hip_bfloat16* __restrict__ Op) {
  __shared__ __attribute__((aligned(16))) char smem[LDS_BYTES];
  const int tid = threadIdx.x, lane = tid & 63;
  const int wid = __builtin_amdgcn_readfirstlane(tid >> 6);
  const int r32 = lane & 31, hi = lane >> 5;
  const int bid = blockIdx.x;
  const int xcd = bid & 7, idx = bid >> 3;
  const int grp = xcd * 8 + (idx >> 2);
  const int g = idx & 3;
  const int qh = grp * 4 + g;
  const char* Kg = (const char*)(Kp + (size_t)grp * SK * D);
  const char* Vg = (const char*)(Vp + (size_t)grp * SK * D);
  const __hip_bfloat16* Qg = Qp + (size_t)qh * SQ * D;

  char* kslot = smem + wid * 2 * SLOT;
  char* vslot = kslot + SLOT;

  const int lr = lane >> 4, lc = lane & 15;
  const unsigned vsrc = lr * 256 + ((lc ^ (lr << 2)) << 4);
  const unsigned ksrc0 = lr * 256 + ((lc ^ lr) << 4);

  auto issue = [&](int t) {
    const size_t off = (size_t)(t * NW + wid) * SLOT;
    const char* kb = Kg + off;
    const char* vb = Vg + off;
#pragma unroll
    for (int n = 0; n < 8; ++n)
      __builtin_amdgcn_global_load_lds((const GLB_AS void*)(kb + n * 1024 + (ksrc0 ^ ((n & 3) << 6))), (LDS_AS void*)(kslot + n * 1024), 16, 0, 2);
#pragma unroll
    for (int n = 0; n < 8; ++n)
      __builtin_amdgcn_global_load_lds((const GLB_AS void*)(vb + n * 1024 + vsrc), (LDS_AS void*)(vslot + n * 1024), 16, 0, 2);
  };

  issue(0);
  bf16x8 qf[8];
#pragma unroll
  for (int s = 0; s < 8; ++s) qf[s] = *(const bf16x8*)(Qg + r32 * D + s * 16 + hi * 8);

  const int kx = hi ^ (r32 & 15);
  const int gq = lane >> 4, i16 = lane & 15, qp = i16 >> 2, pp = i16 & 3;
  const int vrow = 4 * (gq >> 1) + qp;
  const int vch = 2 * (gq & 1) + (pp >> 1);

  f32x16 o[4];
#pragma unroll
  for (int i = 0; i < 4; ++i)
#pragma unroll
    for (int r = 0; r < 16; ++r) o[i][r] = 0.f;
  float m = -__builtin_inff(), l = 0.f;

  for (int t = 0; t < NT; ++t) {
    asm volatile("s_waitcnt vmcnt(0)" ::: "memory");
    bf16x8 kf[8];
#pragma unroll
    for (int s = 0; s < 8; ++s) kf[s] = *(const bf16x8*)(kslot + r32 * 256 + (((2 * s) ^ kx) << 4));
    s16x4 vf[4][2][2];
#pragma unroll
    for (int db = 0; db < 4; ++db)
#pragma unroll
      for (int s = 0; s < 2; ++s)
#pragma unroll
        for (int u = 0; u < 2; ++u) {
          const int row = 16 * s + 8 * u + vrow;
          const int ch = (4 * db + vch) ^ (qp << 2);
          vf[db][s][u] = __builtin_amdgcn_ds_read_tr16_b64_v4i16((LDS_AS s16x4*)(vslot + row * 256 + ch * 16 + 8 * (pp & 1)));
        }
    asm volatile("s_waitcnt lgkmcnt(0)" ::: "memory");
    __builtin_amdgcn_sched_barrier(0);
    if (t + 1 < NT) issue(t + 1);
    __builtin_amdgcn_sched_barrier(0);

    f32x16 sc;
#pragma unroll
    for (int r = 0; r < 16; ++r) sc[r] = 0.f;
#pragma unroll
    for (int s = 0; s < 8; ++s) sc = __builtin_amdgcn_mfma_f32_32x32x16_bf16(kf[s], qf[s], sc, 0, 0, 0);
    float mx = sc[0];
#pragma unroll
    for (int r = 1; r < 16; ++r) mx = fmaxf(mx, sc[r]);
    {
      auto rr = __builtin_amdgcn_permlane32_swap(__float_as_uint(mx), __float_as_uint(mx), false, false);
      mx = fmaxf(__uint_as_float(rr[0]), __uint_as_float(rr[1]));
    }
    const float mn = fmaxf(m, mx);
    const float alpha = __builtin_amdgcn_exp2f((m - mn) * CEXP);
    m = mn;
    const float nm = -mn * CEXP;
    float ps = 0.f;
#pragma unroll
    for (int r = 0; r < 16; ++r) { sc[r] = __builtin_amdgcn_exp2f(fmaf(sc[r], CEXP, nm)); ps += sc[r]; }
    l = l * alpha + ps;
    bf16x8 pf[2];
#pragma unroll
    for (int s = 0; s < 2; ++s) {
      u32x4 w = {pk2(sc[8 * s + 0], sc[8 * s + 1]), pk2(sc[8 * s + 2], sc[8 * s + 3]), pk2(sc[8 * s + 4], sc[8 * s + 5]), pk2(sc[8 * s + 6], sc[8 * s + 7])};
      pf[s] = __builtin_bit_cast(bf16x8, w);
    }
#pragma unroll
    for (int db = 0; db < 4; ++db) {
#pragma unroll
      for (int r = 0; r < 16; ++r) o[db][r] *= alpha;
#pragma unroll
      for (int s = 0; s < 2; ++s) {
        bf16x8 a = __builtin_shufflevector(vf[db][s][0], vf[db][s][1], 0, 1, 2, 3, 4, 5, 6, 7);
        o[db] = __builtin_amdgcn_mfma_f32_32x32x16_bf16(a, pf[s], o[db], 0, 0, 0);
      }
    }
  }

  {
    auto rr = __builtin_amdgcn_permlane32_swap(__float_as_uint(l), __float_as_uint(l), false, false);
    l = __uint_as_float(rr[0]) + __uint_as_float(rr[1]);
  }
  float* stats = (float*)(smem + LDS_STATS);
  if (hi == 0) { stats[wid * 64 + r32] = m; stats[wid * 64 + 32 + r32] = l; }
  __syncthreads();
  float M = stats[r32];
#pragma unroll
  for (int w = 1; w < NW; ++w) M = fmaxf(M, stats[w * 64 + r32]);
  float L = 0.f;
#pragma unroll
  for (int w = 0; w < NW; ++w) L += stats[w * 64 + 32 + r32] * __builtin_amdgcn_exp2f((stats[w * 64 + r32] - M) * CEXP);
  const float f = __builtin_amdgcn_exp2f((m - M) * CEXP) / L;
#pragma unroll
  for (int db = 0; db < 4; ++db)
#pragma unroll
    for (int q4 = 0; q4 < 4; ++q4) {
      f32x4 v = {o[db][4 * q4] * f, o[db][4 * q4 + 1] * f, o[db][4 * q4 + 2] * f, o[db][4 * q4 + 3] * f};
      *(f32x4*)(kslot + (db * 4 + q4) * 1024 + lane * 16) = v;
    }
  __syncthreads();
  __hip_bfloat16* Og = Op + (size_t)qh * SQ * D;
#pragma unroll
  for (int j = 0; j < 16 / NW; ++j) {
    const int qd = wid * (16 / NW) + j;
    f32x4 acc = *(const f32x4*)(smem + qd * 1024 + lane * 16);
#pragma unroll
    for (int w = 1; w < NW; ++w) acc += *(const f32x4*)(smem + w * 2 * SLOT + qd * 1024 + lane * 16);
    u32x2 pkd = {pk2(acc[0], acc[1]), pk2(acc[2], acc[3])};
    *(u32x2*)(Og + r32 * D + (qd >> 2) * 32 + (qd & 3) * 8 + 4 * hi) = pkd;
  }
}

extern "C" void kernel_launch(void* const* d_in, const int* in_sizes, int n_in, void* d_out, int out_size, void* d_ws, size_t ws_size,
                              hipStream_t stream) {
  const __hip_bfloat16* q = (const __hip_bfloat16*)d_in[0];
  const __hip_bfloat16* k = (const __hip_bfloat16*)d_in[1];
  const __hip_bfloat16* v = (const __hip_bfloat16*)d_in[2];
  __hip_bfloat16* o = (__hip_bfloat16*)d_out;
  hipLaunchKernelGGL(gqa_attn_kernel, dim3(Bn * HQ), dim3(64 * NW), 0, stream, q, k, v, o);
}
```

```cpp
#include <hip/hip_runtime.h>
#include <hip/hip_bf16.h>
#include <stdint.h>

typedef __attribute__((ext_vector_type(8))) short bf16x8;
typedef __attribute__((ext_vector_type(4))) short s16x4;
typedef __attribute__((ext_vector_type(16))) float f32x16;
typedef __attribute__((ext_vector_type(4))) float f32x4;
typedef __attribute__((ext_vector_type(2))) float f32x2;
typedef __attribute__((ext_vector_type(2))) __bf16 bf16x2_t;
typedef __attribute__((ext_vector_type(4))) unsigned u32x4;
typedef __attribute__((ext_vector_type(2))) unsigned u32x2;

constexpr int Bn = 8, HQ = 32, HKV = 8, SQ = 32, SK = 2048, D = 128;
constexpr int NSPLIT = 4, KPS = SK / NSPLIT;
constexpr int TK = 64;
constexpr int NT = KPS / TK;
constexpr int NSLOT = 4;
constexpr int HALF = TK * D * 2;
constexpr int SLOT = 2 * HALF;
constexpr int LDS_Q = NSLOT * SLOT;
constexpr int LDS_BYTES = NSLOT * SLOT + 32768;
constexpr int LDS_STATS = 8 * 8192;
constexpr float CEXP = 0.088388347648318447f * 1.4426950408889634f;
constexpr float THR = 10.f;
constexpr size_t WS_PO = 0;
constexpr size_t WS_ST = (size_t)64 * 4 * 4 * 32 * 128 * 2;

#define STPOL 0
#define QDEF
#define LDS_AS __attribute__((address_space(3)))
#define SB() __builtin_amdgcn_sched_barrier(0)

__device__ __forceinline__ unsigned pk2(float a, float b) {
  f32x2 v = {a, b};
  bf16x2_t r = __builtin_convertvector(v, bf16x2_t);
  return __builtin_bit_cast(unsigned, r);
}
__device__ __forceinline__ float bflo(unsigned u) { return __uint_as_float(u << 16); }
__device__ __forceinline__ float bfhi(unsigned u) { return __uint_as_float(u & 0xffff0000u); }

__device__ __forceinline__ void dma2k(__amdgpu_buffer_rsrc_t rsrc, unsigned voff, unsigned soff, unsigned ldsaddr) {
  unsigned keep;
  asm volatile("s_mov_b32 %0, m0\n\ts_mov_b32 m0, %1\n\ts_nop 0\n\tbuffer_load_dwordx4 %2, %3, %4 offen nt lds\n\t"
               "s_add_u32 m0, m0, 0x380\n\ts_nop 0\n\tbuffer_load_dwordx4 %2, %3, %4 offen offset:128 nt lds\n\ts_mov_b32 m0, %0"
               : "=&s"(keep) : "s"(ldsaddr), "v"(voff), "s"(rsrc), "s"(soff) : "memory", "scc");
}

__device__ __forceinline__ void dma2k_def(__amdgpu_buffer_rsrc_t rsrc, unsigned voff, unsigned soff, unsigned ldsaddr) {
  unsigned keep;
  asm volatile("s_mov_b32 %0, m0\n\ts_mov_b32 m0, %1\n\ts_nop 0\n\tbuffer_load_dwordx4 %2, %3, %4 offen lds\n\t"
               "s_add_u32 m0, m0, 0x380\n\ts_nop 0\n\tbuffer_load_dwordx4 %2, %3, %4 offen offset:128 lds\n\ts_mov_b32 m0, %0"
               : "=&s"(keep) : "s"(ldsaddr), "v"(voff), "s"(rsrc), "s"(soff) : "memory", "scc");
}

#define ST(i)
template <int N> __device__ __forceinline__ void wait_bar() {
  if constexpr (N == 8) asm volatile("s_waitcnt vmcnt(8) lgkmcnt(0)\n\ts_barrier" ::: "memory");
  else if constexpr (N == 6) asm volatile("s_waitcnt vmcnt(6) lgkmcnt(0)\n\ts_barrier" ::: "memory");
  else if constexpr (N == 2) asm volatile("s_waitcnt vmcnt(2) lgkmcnt(0)\n\ts_barrier" ::: "memory");
  else if constexpr (N == 4) asm volatile("s_waitcnt vmcnt(4) lgkmcnt(0)\n\ts_barrier" ::: "memory");
  else asm volatile("s_waitcnt vmcnt(0) lgkmcnt(0)\n\ts_barrier" ::: "memory");
}

__global__ __launch_bounds__(512, 2) void gqa_split_kernel(const __hip_bfloat16* __restrict__ Qp, const __hip_bfloat16* __restrict__ Kp,
                                                           const __hip_bfloat16* __restrict__ Vp, char* __restrict__ ws) {
  __shared__ __attribute__((aligned(16))) char smem[LDS_BYTES];
  ST(0);
  const int tid = threadIdx.x, lane = tid & 63;
  const int wid = __builtin_amdgcn_readfirstlane(tid >> 6);
  const int g = wid & 3, kh = wid >> 2;
  const int r32 = lane & 31, hi = lane >> 5;
  const int bid = blockIdx.x;
  const int grp = bid >> 2, split = bid & 3;
  const size_t kvoff = ((size_t)grp * SK + (size_t)split * KPS) * D;
  __amdgpu_buffer_rsrc_t rK = __builtin_amdgcn_make_buffer_rsrc((void*)(Kp + kvoff), 0, KPS * D * 2, 0x00020000);
  __amdgpu_buffer_rsrc_t rV = __builtin_amdgcn_make_buffer_rsrc((void*)(Vp + kvoff), 0, KPS * D * 2, 0x00020000);
  const unsigned ldsbase = (unsigned)(uintptr_t)(LDS_AS char*)smem;

  const int dsub = lane >> 5, drr = (lane >> 2) & 7, dx = lane & 3;
  const unsigned dsrc = (8 * wid + drr) * 256 + (4 * dsub + (dx ^ ((2 * wid + (drr >> 2)) & 3))) * 16;
  auto issue = [&](int j) {
    const unsigned lb = ldsbase + (j % NSLOT) * SLOT + wid * 2048;
    if (j + 1 < NT) dma2k(rK, dsrc, (j + 1) * HALF, lb);
    dma2k(rV, dsrc, j * HALF, lb + HALF);
  };
  __amdgpu_buffer_rsrc_t rQ = __builtin_amdgcn_make_buffer_rsrc((void*)(Qp + (size_t)grp * 4 * SQ * D), 0, 4 * SQ * D * 2, 0x00020000);

  dma2k(rK, dsrc, 0, ldsbase + (NSLOT - 1) * SLOT + wid * 2048);
  __builtin_amdgcn_s_barrier();
  dma2k_def(rQ, dsrc, 0, ldsbase + LDS_Q + wid * 2048);
  dma2k_def(rQ, dsrc, HALF, ldsbase + LDS_Q + HALF + wid * 2048);
  __builtin_amdgcn_s_barrier();
  issue(0);
  __builtin_amdgcn_s_barrier();
  issue(1);
  ST(1);

  const int krow = 32 * kh + r32;
  const int ka0 = 2048 * (krow >> 3) + 64 * (r32 & 7) + 16 * ((0 + hi) ^ ((r32 >> 2) & 3));
  const int ka1 = 2048 * (krow >> 3) + 64 * (r32 & 7) + 16 * ((2 + hi) ^ ((r32 >> 2) & 3));
  const int gq = lane >> 4, i16 = lane & 15, qp = i16 >> 2, pp = i16 & 3;
  const int vh = gq >> 1, vc = 2 * (gq & 1) + (pp >> 1);
  const int va0 = HALF + 8192 * kh + 64 * (4 * vh + qp) + 16 * (vc ^ vh) + 8 * (pp & 1);
  const int va1 = HALF + 8192 * kh + 2048 + 64 * (4 * vh + qp) + 16 * (vc ^ (2 + vh)) + 8 * (pp & 1);

  f32x16 o[4];
#pragma unroll
  for (int i = 0; i < 4; ++i)
#pragma unroll
    for (int r = 0; r < 16; ++r) o[i][r] = 0.f;
  float m = -__builtin_inff(), l = 0.f;
  f32x16 negm;
#pragma unroll
  for (int r = 0; r < 16; ++r) negm[r] = 0.f;
  float mref = 0.f;

  bf16x8 qf[8];
  bf16x8 kf[8];
  bf16x8 vf[4][2];
  auto ldk = [&](int t) {
    const char* sp = smem + ((t + NSLOT - 1) % NSLOT) * SLOT;
#pragma unroll
    for (int s = 0; s < 8; ++s) kf[s] = *(const bf16x8*)(sp + ((s & 1) ? ka1 : ka0) + 512 * (s >> 1));
  };
  auto ldv = [&](int t) {
    const char* sp = smem + (t % NSLOT) * SLOT;
#pragma unroll
    for (int s = 0; s < 2; ++s)
#pragma unroll
      for (int db = 0; db < 4; ++db) {
        s16x4 v0 = __builtin_amdgcn_ds_read_tr16_b64_v4i16((LDS_AS s16x4*)(sp + va0 + 4096 * s + 512 * db));
        s16x4 v1 = __builtin_amdgcn_ds_read_tr16_b64_v4i16((LDS_AS s16x4*)(sp + va1 + 4096 * s + 512 * db));
        vf[db][s] = __builtin_shufflevector(v0, v1, 0, 1, 2, 3, 4, 5, 6, 7);
      }
  };
  auto qk = [&]() {
    f32x16 sc = __builtin_amdgcn_mfma_f32_32x32x16_bf16(kf[0], qf[0], negm, 0, 0, 0);
#pragma unroll
    for (int s = 1; s < 8; ++s) sc = __builtin_amdgcn_mfma_f32_32x32x16_bf16(kf[s], qf[s], sc, 0, 0, 0);
    return sc;
  };
  auto maxcheck = [&](f32x16& sc, float mS) {
    float mx = sc[0];
#pragma unroll
    for (int r = 1; r < 16; ++r) mx = fmaxf(mx, sc[r]);
    {
      auto rr = __builtin_amdgcn_permlane32_swap(__float_as_uint(mx), __float_as_uint(mx), false, false);
      mx = fmaxf(__uint_as_float(rr[0]), __uint_as_float(rr[1]));
    }
    const bool slow = !(mx <= THR) || (mS != m);
    if (__builtin_expect(__builtin_amdgcn_ballot_w64(slow) != 0, 0)) {
      const float mn = fmaxf(m, mx + mS);
      const float alpha = __builtin_amdgcn_exp2f(m - mn);
      const float dlt = mS - mn;
      m = mn; mref = mn;
      l *= alpha;
#pragma unroll
      for (int r = 0; r < 16; ++r) { sc[r] += dlt; negm[r] = -mn; }
#pragma unroll
      for (int db = 0; db < 4; ++db)
#pragma unroll
        for (int r = 0; r < 16; ++r) o[db][r] *= alpha;
    }
  };
  bf16x8 pf[2];
  auto expo = [&](f32x16& sc) {
    float ps = 0.f;
#pragma unroll
    for (int r = 0; r < 16; ++r) { sc[r] = __builtin_amdgcn_exp2f(sc[r]); ps += sc[r]; }
    l += ps;
#pragma unroll
    for (int s = 0; s < 2; ++s) {
      u32x4 w = {pk2(sc[8 * s + 0], sc[8 * s + 1]), pk2(sc[8 * s + 2], sc[8 * s + 3]), pk2(sc[8 * s + 4], sc[8 * s + 5]), pk2(sc[8 * s + 6], sc[8 * s + 7])};
      pf[s] = __builtin_bit_cast(bf16x8, w);
    }
  };
  auto pv = [&]() {
#pragma unroll
    for (int s = 0; s < 2; ++s)
#pragma unroll
      for (int db = 0; db < 4; ++db) o[db] = __builtin_amdgcn_mfma_f32_32x32x16_bf16(vf[db][s], pf[s], o[db], 0, 0, 0);
  };

  ST(2);
  wait_bar<8>();
  ST(3);
  {
    const int qrow = 32 * g + r32;
    const char* qp_ = smem + LDS_Q + 2048 * (qrow >> 3) + 64 * (r32 & 7);
#pragma unroll
    for (int s = 0; s < 8; ++s) {
      u32x4 raw = *(const u32x4*)(qp_ + 16 * ((2 * (s & 1) + hi) ^ ((r32 >> 2) & 3)) + 512 * (s >> 1));
      u32x4 w;
#pragma unroll
      for (int j = 0; j < 4; ++j) w[j] = pk2(bflo(raw[j]) * CEXP, bfhi(raw[j]) * CEXP);
      qf[s] = __builtin_bit_cast(bf16x8, w);
    }
  }
  ldk(0);
  f32x16 scur = qk();
  float mScur = mref;
  issue(2);
#pragma unroll
  for (int t = 0; t < NT - 1; ++t) {
    ST(4 + 2 * t);
    if (t < NT - 3) wait_bar<8>(); else if (t == NT - 3) wait_bar<6>(); else wait_bar<2>();
    ST(5 + 2 * t);
    if (t + 3 < NT) issue(t + 3);
    ldk(t + 1);
    SB();
    if (t > 0) pv();
    SB();
    ldv(t);
    SB();
    maxcheck(scur, mScur);
    SB();
    const float mSn = mref;
    f32x16 snext = qk();
    expo(scur);
#pragma unroll
    for (int i = 0; i < 8; ++i) {
      __builtin_amdgcn_sched_group_barrier(0x8, 1, 0);
      __builtin_amdgcn_sched_group_barrier(0x402, 5, 0);
    }
    SB();
    scur = snext; mScur = mSn;
  }
  pv();
  SB();
  maxcheck(scur, mScur);
  expo(scur);
  ST(18);
  wait_bar<0>();
  ldv(NT - 1);
  pv();
  ST(19);

  {
    auto rr = __builtin_amdgcn_permlane32_swap(__float_as_uint(l), __float_as_uint(l), false, false);
    l = __uint_as_float(rr[0]) + __uint_as_float(rr[1]);
  }
  __syncthreads();
  float* stats = (float*)(smem + LDS_STATS);
  if (hi == 0) { stats[wid * 64 + r32] = m; stats[wid * 64 + 32 + r32] = l; }
  char* xb = smem + wid * 8192;
#pragma unroll
  for (int j = 0; j < 2; ++j)
#pragma unroll
    for (int q4 = 0; q4 < 4; ++q4) {
      const f32x16& src = kh ? o[j] : o[2 + j];
      f32x4 v = {src[4 * q4], src[4 * q4 + 1], src[4 * q4 + 2], src[4 * q4 + 3]};
      *(f32x4*)(xb + (j * 4 + q4) * 1024 + lane * 16) = v;
    }
  __syncthreads();
  const int pw = wid ^ 4;
  const float m2 = stats[pw * 64 + r32], l2 = stats[pw * 64 + 32 + r32];
  const float M = fmaxf(m, m2);
  const float e1 = __builtin_amdgcn_exp2f(m - M), e2 = __builtin_amdgcn_exp2f(m2 - M);
  const float L = l * e1 + l2 * e2;
  const float rL = 1.f / L;
  const float f1 = e1 * rL, f2 = e2 * rL;
  const char* pb = smem + pw * 8192;
  __amdgpu_buffer_rsrc_t rW = __builtin_amdgcn_make_buffer_rsrc((void*)ws, 0, 16 << 20, 0x00020000);
  char* po = ws + WS_PO + ((size_t)(bid * 4 + g)) * (32 * 128 * 2);
#pragma unroll
  for (int j = 0; j < 2; ++j) {
    const f32x16& own = kh ? o[2 + j] : o[j];
#pragma unroll
    for (int pr = 0; pr < 2; ++pr) {
      f32x4 a = *(const f32x4*)(pb + (j * 4 + 2 * pr) * 1024 + lane * 16);
      f32x4 b = *(const f32x4*)(pb + (j * 4 + 2 * pr + 1) * 1024 + lane * 16);
      u32x4 w = {pk2(own[8 * pr + 0] * f1 + a[0] * f2, own[8 * pr + 1] * f1 + a[1] * f2), pk2(own[8 * pr + 2] * f1 + a[2] * f2, own[8 * pr + 3] * f1 + a[3] * f2),
                 pk2(own[8 * pr + 4] * f1 + b[0] * f2, own[8 * pr + 5] * f1 + b[1] * f2), pk2(own[8 * pr + 6] * f1 + b[2] * f2, own[8 * pr + 7] * f1 + b[3] * f2)};
      const int db = 2 * kh + j;
      *(u32x4*)(po + (db * 2 + pr) * 1024 + lane * 16) = w;
    }
  }
  if (kh == 0 && hi == 0) {
    float* st = (float*)(ws + WS_ST) + (size_t)(bid * 4 + g) * 64;
    st[r32] = M; st[32 + r32] = L;
  }
}

__global__ __launch_bounds__(256) void gqa_combine_kernel(const char* __restrict__ ws, __hip_bfloat16* __restrict__ Op) {
  const int tid = threadIdx.x, lane = tid & 63;
  const int wv = blockIdx.x * 4 + (tid >> 6);
  const int part = wv & 7, gh = wv >> 3;
  const int grp = gh >> 2, g = gh & 3;
  const int r32 = lane & 31, hi = lane >> 5;
  u32x4 p[4]; float ms[4], ls[4];
#pragma unroll
  for (int s = 0; s < 4; ++s) {
    const size_t u = (size_t)((grp * 4 + s) * 4 + g);
    p[s] = *(const u32x4*)(ws + WS_PO + u * (32 * 128 * 2) + part * 1024 + lane * 16);
    const float* st = (const float*)(ws + WS_ST) + u * 64;
    ms[s] = st[r32]; ls[s] = st[32 + r32];
  }
  const float M = fmaxf(fmaxf(ms[0], ms[1]), fmaxf(ms[2], ms[3]));
  float w[4], W = 0.f;
#pragma unroll
  for (int s = 0; s < 4; ++s) { w[s] = ls[s] * __builtin_amdgcn_exp2f(ms[s] - M); W += w[s]; }
  const float rW = 1.f / W;
  float acc[8];
#pragma unroll
  for (int j = 0; j < 8; ++j) acc[j] = 0.f;
#pragma unroll
  for (int s = 0; s < 4; ++s) {
    const float ww = w[s] * rW;
#pragma unroll
    for (int j = 0; j < 4; ++j) { acc[2 * j] += ww * bflo(p[s][j]); acc[2 * j + 1] += ww * bfhi(p[s][j]); }
  }
  const int db = part >> 1, pr = part & 1;
  __hip_bfloat16* orow = Op + ((size_t)gh * SQ + r32) * D + db * 32 + pr * 16 + 4 * hi;
  u32x2 a = {pk2(acc[0], acc[1]), pk2(acc[2], acc[3])};
  u32x2 b = {pk2(acc[4], acc[5]), pk2(acc[6], acc[7])};
  *(u32x2*)(orow) = a;
  *(u32x2*)(orow + 8) = b;
}

extern "C" void kernel_launch(void* const* d_in, const int* in_sizes, int n_in, void* d_out, int out_size, void* d_ws, size_t ws_size,
                              hipStream_t stream) {
  const __hip_bfloat16* q = (const __hip_bfloat16*)d_in[0];
  const __hip_bfloat16* k = (const __hip_bfloat16*)d_in[1];
  const __hip_bfloat16* v = (const __hip_bfloat16*)d_in[2];
  __hip_bfloat16* o = (__hip_bfloat16*)d_out;
  hipLaunchKernelGGL(gqa_split_kernel, dim3(256), dim3(512), 0, stream, q, k, v, (char*)d_ws);
  hipLaunchKernelGGL(gqa_combine_kernel, dim3(512), dim3(256), 0, stream, (const char*)d_ws, o);
}
```

```cpp
#include <hip/hip_runtime.h>
#include <hip/hip_bf16.h>
#include <stdint.h>

typedef __attribute__((ext_vector_type(8))) short bf16x8;
typedef __attribute__((ext_vector_type(4))) short s16x4;
typedef __attribute__((ext_vector_type(16))) float f32x16;
typedef __attribute__((ext_vector_type(4))) float f32x4;
typedef __attribute__((ext_vector_type(2))) float f32x2;
typedef __attribute__((ext_vector_type(2))) __bf16 bf16x2_t;
typedef __attribute__((ext_vector_type(4))) unsigned u32x4;
typedef __attribute__((ext_vector_type(2))) unsigned u32x2;

constexpr int Bn = 8, HQ = 32, HKV = 8, SQ = 32, SK = 2048, D = 128;
constexpr int NSPLIT = 4, KPS = SK / NSPLIT;
constexpr int TK = 64;
constexpr int NT = KPS / TK;
constexpr int NSLOT = 4;
constexpr int HALF = TK * D * 2;
constexpr int SLOT = 2 * HALF;
constexpr int LDS_Q = NSLOT * SLOT;
constexpr int LDS_BYTES = NSLOT * SLOT + 32768;
constexpr int LDS_STATS = 8 * 8192;
constexpr float CEXP = 0.088388347648318447f * 1.4426950408889634f;
constexpr float THR = 10.f;
constexpr size_t WS_PO = 0;
constexpr size_t WS_ST = (size_t)64 * 4 * 4 * 32 * 128 * 2;

#define STPOL 0
#define LDS_AS __attribute__((address_space(3)))
#define SB() __builtin_amdgcn_sched_barrier(0)

__device__ __forceinline__ unsigned pk2(float a, float b) {
  f32x2 v = {a, b};
  bf16x2_t r = __builtin_convertvector(v, bf16x2_t);
  return __builtin_bit_cast(unsigned, r);
}
__device__ __forceinline__ float bflo(unsigned u) { return __uint_as_float(u << 16); }
__device__ __forceinline__ float bfhi(unsigned u) { return __uint_as_float(u & 0xffff0000u); }

__device__ __forceinline__ void dma2k(__amdgpu_buffer_rsrc_t rsrc, unsigned voff, unsigned soff, unsigned ldsaddr) {
  unsigned keep;
  asm volatile("s_mov_b32 %0, m0\n\ts_mov_b32 m0, %1\n\ts_nop 0\n\tbuffer_load_dwordx4 %2, %3, %4 offen lds\n\t"
               "s_add_u32 m0, m0, 0x380\n\ts_nop 0\n\tbuffer_load_dwordx4 %2, %3, %4 offen offset:128 lds\n\ts_mov_b32 m0, %0"
               : "=&s"(keep) : "s"(ldsaddr), "v"(voff), "s"(rsrc), "s"(soff) : "memory", "scc");
}

__device__ __forceinline__ void dma2k_def(__amdgpu_buffer_rsrc_t rsrc, unsigned voff, unsigned soff, unsigned ldsaddr) {
  unsigned keep;
  asm volatile("s_mov_b32 %0, m0\n\ts_mov_b32 m0, %1\n\ts_nop 0\n\tbuffer_load_dwordx4 %2, %3, %4 offen lds\n\t"
               "s_add_u32 m0, m0, 0x380\n\ts_nop 0\n\tbuffer_load_dwordx4 %2, %3, %4 offen offset:128 lds\n\ts_mov_b32 m0, %0"
               : "=&s"(keep) : "s"(ldsaddr), "v"(voff), "s"(rsrc), "s"(soff) : "memory", "scc");
}

#define ST(i)
template <int N> __device__ __forceinline__ void wait_bar() {
  if constexpr (N == 8) asm volatile("s_waitcnt vmcnt(8) lgkmcnt(0)\n\ts_barrier" ::: "memory");
  else if constexpr (N == 6) asm volatile("s_waitcnt vmcnt(6) lgkmcnt(0)\n\ts_barrier" ::: "memory");
  else if constexpr (N == 2) asm volatile("s_waitcnt vmcnt(2) lgkmcnt(0)\n\ts_barrier" ::: "memory");
  else if constexpr (N == 4) asm volatile("s_waitcnt vmcnt(4) lgkmcnt(0)\n\ts_barrier" ::: "memory");
  else asm volatile("s_waitcnt vmcnt(0) lgkmcnt(0)\n\ts_barrier" ::: "memory");
}

__global__ __launch_bounds__(512, 2) void gqa_split_kernel(const __hip_bfloat16* __restrict__ Qp, const __hip_bfloat16* __restrict__ Kp,
                                                           const __hip_bfloat16* __restrict__ Vp, char* __restrict__ ws) {
  __shared__ __attribute__((aligned(16))) char smem[LDS_BYTES];
  ST(0);
  const int tid = threadIdx.x, lane = tid & 63;
  const int wid = __builtin_amdgcn_readfirstlane(tid >> 6);
  const int g = wid & 3, kh = wid >> 2;
  const int r32 = lane & 31, hi = lane >> 5;
  const int bid = blockIdx.x;
  const int grp = bid >> 2, split = bid & 3;
  const size_t kvoff = ((size_t)grp * SK + (size_t)split * KPS) * D;
  __amdgpu_buffer_rsrc_t rK = __builtin_amdgcn_make_buffer_rsrc((void*)(Kp + kvoff), 0, KPS * D * 2, 0x00020000);
  __amdgpu_buffer_rsrc_t rV = __builtin_amdgcn_make_buffer_rsrc((void*)(Vp + kvoff), 0, KPS * D * 2, 0x00020000);
  const unsigned ldsbase = (unsigned)(uintptr_t)(LDS_AS char*)smem;

  const int dsub = lane >> 5, drr = (lane >> 2) & 7, dx = lane & 3;
  const unsigned dsrc = (8 * wid + drr) * 256 + (4 * dsub + (dx ^ ((2 * wid + (drr >> 2)) & 3))) * 16;
  auto issue = [&](int j) {
    const unsigned lb = ldsbase + (j % NSLOT) * SLOT + wid * 2048;
    if (j + 1 < NT) dma2k(rK, dsrc, (j + 1) * HALF, lb);
    dma2k(rV, dsrc, j * HALF, lb + HALF);
  };
  __amdgpu_buffer_rsrc_t rQ = __builtin_amdgcn_make_buffer_rsrc((void*)(Qp + (size_t)grp * 4 * SQ * D), 0, 4 * SQ * D * 2, 0x00020000);

  dma2k(rK, dsrc, 0, ldsbase + (NSLOT - 1) * SLOT + wid * 2048);
  __builtin_amdgcn_s_barrier();
  dma2k(rQ, dsrc, 0, ldsbase + LDS_Q + wid * 2048);
  dma2k(rQ, dsrc, HALF, ldsbase + LDS_Q + HALF + wid * 2048);
  __builtin_amdgcn_s_barrier();
  issue(0);
  __builtin_amdgcn_s_barrier();
  issue(1);
  ST(1);

  const int krow = 32 * kh + r32;
  const int ka0 = 2048 * (krow >> 3) + 64 * (r32 & 7) + 16 * ((0 + hi) ^ ((r32 >> 2) & 3));
  const int ka1 = 2048 * (krow >> 3) + 64 * (r32 & 7) + 16 * ((2 + hi) ^ ((r32 >> 2) & 3));
  const int gq = lane >> 4, i16 = lane & 15, qp = i16 >> 2, pp = i16 & 3;
  const int vh = gq >> 1, vc = 2 * (gq & 1) + (pp >> 1);
  const int va0 = HALF + 8192 * kh + 64 * (4 * vh + qp) + 16 * (vc ^ vh) + 8 * (pp & 1);
  const int va1 = HALF + 8192 * kh + 2048 + 64 * (4 * vh + qp) + 16 * (vc ^ (2 + vh)) + 8 * (pp & 1);

  f32x16 o[4];
#pragma unroll
  for (int i = 0; i < 4; ++i)
#pragma unroll
    for (int r = 0; r < 16; ++r) o[i][r] = 0.f;
  float m = -__builtin_inff(), l = 0.f;
  f32x16 negm;
#pragma unroll
  for (int r = 0; r < 16; ++r) negm[r] = 0.f;
  float mref = 0.f;

  bf16x8 qf[8];
  bf16x8 kf[8];
  bf16x8 vf[4][2];
  auto ldk = [&](int t) {
    const char* sp = smem + ((t + NSLOT - 1) % NSLOT) * SLOT;
#pragma unroll
    for (int s = 0; s < 8; ++s) kf[s] = *(const bf16x8*)(sp + ((s & 1) ? ka1 : ka0) + 512 * (s >> 1));
  };
  auto ldv = [&](int t) {
    const char* sp = smem + (t % NSLOT) * SLOT;
#pragma unroll
    for (int s = 0; s < 2; ++s)
#pragma unroll
      for (int db = 0; db < 4; ++db) {
        s16x4 v0 = __builtin_amdgcn_ds_read_tr16_b64_v4i16((LDS_AS s16x4*)(sp + va0 + 4096 * s + 512 * db));
        s16x4 v1 = __builtin_amdgcn_ds_read_tr16_b64_v4i16((LDS_AS s16x4*)(sp + va1 + 4096 * s + 512 * db));
        vf[db][s] = __builtin_shufflevector(v0, v1, 0, 1, 2, 3, 4, 5, 6, 7);
      }
  };
  auto qk = [&]() {
    f32x16 sc = __builtin_amdgcn_mfma_f32_32x32x16_bf16(kf[0], qf[0], negm, 0, 0, 0);
#pragma unroll
    for (int s = 1; s < 8; ++s) sc = __builtin_amdgcn_mfma_f32_32x32x16_bf16(kf[s], qf[s], sc, 0, 0, 0);
    return sc;
  };
  auto maxcheck = [&](f32x16& sc, float mS) {
    float mx = sc[0];
#pragma unroll
    for (int r = 1; r < 16; ++r) mx = fmaxf(mx, sc[r]);
    {
      auto rr = __builtin_amdgcn_permlane32_swap(__float_as_uint(mx), __float_as_uint(mx), false, false);
      mx = fmaxf(__uint_as_float(rr[0]), __uint_as_float(rr[1]));
    }
    const bool slow = !(mx <= THR) || (mS != m);
    if (__builtin_expect(__builtin_amdgcn_ballot_w64(slow) != 0, 0)) {
      const float mn = fmaxf(m, mx + mS);
      const float alpha = __builtin_amdgcn_exp2f(m - mn);
      const float dlt = mS - mn;
      m = mn; mref = mn;
      l *= alpha;
#pragma unroll
      for (int r = 0; r < 16; ++r) { sc[r] += dlt; negm[r] = -mn; }
#pragma unroll
      for (int db = 0; db < 4; ++db)
#pragma unroll
        for (int r = 0; r < 16; ++r) o[db][r] *= alpha;
    }
  };
  bf16x8 pf[2];
  auto expo = [&](f32x16& sc) {
    float ps = 0.f;
#pragma unroll
    for (int r = 0; r < 16; ++r) { sc[r] = __builtin_amdgcn_exp2f(sc[r]); ps += sc[r]; }
    l += ps;
#pragma unroll
    for (int s = 0; s < 2; ++s) {
      u32x4 w = {pk2(sc[8 * s + 0], sc[8 * s + 1]), pk2(sc[8 * s + 2], sc[8 * s + 3]), pk2(sc[8 * s + 4], sc[8 * s + 5]), pk2(sc[8 * s + 6], sc[8 * s + 7])};
      pf[s] = __builtin_bit_cast(bf16x8, w);
    }
  };
  auto pv = [&]() {
#pragma unroll
    for (int s = 0; s < 2; ++s)
#pragma unroll
      for (int db = 0; db < 4; ++db) o[db] = __builtin_amdgcn_mfma_f32_32x32x16_bf16(vf[db][s], pf[s], o[db], 0, 0, 0);
  };

  ST(2);
  wait_bar<8>();
  ST(3);
  {
    const int qrow = 32 * g + r32;
    const char* qp_ = smem + LDS_Q + 2048 * (qrow >> 3) + 64 * (r32 & 7);
#pragma unroll
    for (int s = 0; s < 8; ++s) {
      u32x4 raw = *(const u32x4*)(qp_ + 16 * ((2 * (s & 1) + hi) ^ ((r32 >> 2) & 3)) + 512 * (s >> 1));
      u32x4 w;
#pragma unroll
      for (int j = 0; j < 4; ++j) w[j] = pk2(bflo(raw[j]) * CEXP, bfhi(raw[j]) * CEXP);
      qf[s] = __builtin_bit_cast(bf16x8, w);
    }
  }
  ldk(0);
  f32x16 scur = qk();
  float mScur = mref;
  issue(2);
#pragma unroll
  for (int t = 0; t < NT - 1; ++t) {
    ST(4 + 2 * t);
    if (t < NT - 3) wait_bar<8>(); else if (t == NT - 3) wait_bar<6>(); else wait_bar<2>();
    ST(5 + 2 * t);
    if (t + 3 < NT) issue(t + 3);
    ldk(t + 1);
    SB();
    if (t > 0) pv();
    SB();
    ldv(t);
    SB();
    maxcheck(scur, mScur);
    SB();
    const float mSn = mref;
    f32x16 snext = qk();
    expo(scur);
#pragma unroll
    for (int i = 0; i < 8; ++i) {
      __builtin_amdgcn_sched_group_barrier(0x8, 1, 0);
      __builtin_amdgcn_sched_group_barrier(0x402, 5, 0);
    }
    SB();
    scur = snext; mScur = mSn;
  }
  pv();
  SB();
  maxcheck(scur, mScur);
  expo(scur);
  ST(18);
  wait_bar<0>();
  ldv(NT - 1);
  pv();
  ST(19);

  {
    auto rr = __builtin_amdgcn_permlane32_swap(__float_as_uint(l), __float_as_uint(l), false, false);
    l = __uint_as_float(rr[0]) + __uint_as_float(rr[1]);
  }
  __syncthreads();
  float* stats = (float*)(smem + LDS_STATS);
  if (hi == 0) { stats[wid * 64 + r32] = m; stats[wid * 64 + 32 + r32] = l; }
  char* xb = smem + wid * 8192;
#pragma unroll
  for (int j = 0; j < 2; ++j)
#pragma unroll
    for (int q4 = 0; q4 < 4; ++q4) {
      const f32x16& src = kh ? o[j] : o[2 + j];
      f32x4 v = {src[4 * q4], src[4 * q4 + 1], src[4 * q4 + 2], src[4 * q4 + 3]};
      *(f32x4*)(xb + (j * 4 + q4) * 1024 + lane * 16) = v;
    }
  __syncthreads();
  const int pw = wid ^ 4;
  const float m2 = stats[pw * 64 + r32], l2 = stats[pw * 64 + 32 + r32];
  const float M = fmaxf(m, m2);
  const float e1 = __builtin_amdgcn_exp2f(m - M), e2 = __builtin_amdgcn_exp2f(m2 - M);
  const float L = l * e1 + l2 * e2;
  const float rL = 1.f / L;
  const float f1 = e1 * rL, f2 = e2 * rL;
  const char* pb = smem + pw * 8192;
  __amdgpu_buffer_rsrc_t rW = __builtin_amdgcn_make_buffer_rsrc((void*)ws, 0, 16 << 20, 0x00020000);
  char* po = ws + WS_PO + ((size_t)(bid * 4 + g)) * (32 * 128 * 2);
#pragma unroll
  for (int j = 0; j < 2; ++j) {
    const f32x16& own = kh ? o[2 + j] : o[j];
#pragma unroll
    for (int pr = 0; pr < 2; ++pr) {
      f32x4 a = *(const f32x4*)(pb + (j * 4 + 2 * pr) * 1024 + lane * 16);
      f32x4 b = *(const f32x4*)(pb + (j * 4 + 2 * pr + 1) * 1024 + lane * 16);
      u32x4 w = {pk2(own[8 * pr + 0] * f1 + a[0] * f2, own[8 * pr + 1] * f1 + a[1] * f2), pk2(own[8 * pr + 2] * f1 + a[2] * f2, own[8 * pr + 3] * f1 + a[3] * f2),
                 pk2(own[8 * pr + 4] * f1 + b[0] * f2, own[8 * pr + 5] * f1 + b[1] * f2), pk2(own[8 * pr + 6] * f1 + b[2] * f2, own[8 * pr + 7] * f1 + b[3] * f2)};
      const int db = 2 * kh + j;
      *(u32x4*)(po + (db * 2 + pr) * 1024 + lane * 16) = w;
    }
  }
  if (kh == 0 && hi == 0) {
    float* st = (float*)(ws + WS_ST) + (size_t)(bid * 4 + g) * 64;
    st[r32] = M; st[32 + r32] = L;
  }
}

__global__ __launch_bounds__(256) void gqa_combine_kernel(const char* __restrict__ ws, __hip_bfloat16* __restrict__ Op) {
  const int tid = threadIdx.x, lane = tid & 63;
  const int wv = blockIdx.x * 4 + (tid >> 6);
  const int part = wv & 7, gh = wv >> 3;
  const int grp = gh >> 2, g = gh & 3;
  const int r32 = lane & 31, hi = lane >> 5;
  u32x4 p[4]; float ms[4], ls[4];
#pragma unroll
  for (int s = 0; s < 4; ++s) {
    const size_t u = (size_t)((grp * 4 + s) * 4 + g);
    p[s] = *(const u32x4*)(ws + WS_PO + u * (32 * 128 * 2) + part * 1024 + lane * 16);
    const float* st = (const float*)(ws + WS_ST) + u * 64;
    ms[s] = st[r32]; ls[s] = st[32 + r32];
  }
  const float M = fmaxf(fmaxf(ms[0], ms[1]), fmaxf(ms[2], ms[3]));
  float w[4], W = 0.f;
#pragma unroll
  for (int s = 0; s < 4; ++s) { w[s] = ls[s] * __builtin_amdgcn_exp2f(ms[s] - M); W += w[s]; }
  const float rW = 1.f / W;
  float acc[8];
#pragma unroll
  for (int j = 0; j < 8; ++j) acc[j] = 0.f;
#pragma unroll
  for (int s = 0; s < 4; ++s) {
    const float ww = w[s] * rW;
#pragma unroll
    for (int j = 0; j < 4; ++j) { acc[2 * j] += ww * bflo(p[s][j]); acc[2 * j + 1] += ww * bfhi(p[s][j]); }
  }
  const int db = part >> 1, pr = part & 1;
  __hip_bfloat16* orow = Op + ((size_t)gh * SQ + r32) * D + db * 32 + pr * 16 + 4 * hi;
  u32x2 a = {pk2(acc[0], acc[1]), pk2(acc[2], acc[3])};
  u32x2 b = {pk2(acc[4], acc[5]), pk2(acc[6], acc[7])};
  *(u32x2*)(orow) = a;
  *(u32x2*)(orow + 8) = b;
}

extern "C" void kernel_launch(void* const* d_in, const int* in_sizes, int n_in, void* d_out, int out_size, void* d_ws, size_t ws_size,
                              hipStream_t stream) {
  const __hip_bfloat16* q = (const __hip_bfloat16*)d_in[0];
  const __hip_bfloat16* k = (const __hip_bfloat16*)d_in[1];
  const __hip_bfloat16* v = (const __hip_bfloat16*)d_in[2];
  __hip_bfloat16* o = (__hip_bfloat16*)d_out;
  hipLaunchKernelGGL(gqa_split_kernel, dim3(256), dim3(512), 0, stream, q, k, v, (char*)d_ws);
  hipLaunchKernelGGL(gqa_combine_kernel, dim3(512), dim3(256), 0, stream, (const char*)d_ws, o);
}
```

```cpp
#include <hip/hip_runtime.h>
#include <hip/hip_bf16.h>
#include <stdint.h>

typedef __attribute__((ext_vector_type(8))) short bf16x8;
typedef __attribute__((ext_vector_type(4))) short s16x4;
typedef __attribute__((ext_vector_type(16))) float f32x16;
typedef __attribute__((ext_vector_type(4))) float f32x4;
typedef __attribute__((ext_vector_type(2))) float f32x2;
typedef __attribute__((ext_vector_type(2))) __bf16 bf16x2_t;
typedef __attribute__((ext_vector_type(4))) unsigned u32x4;
typedef __attribute__((ext_vector_type(2))) unsigned u32x2;

constexpr int Bn = 8, HQ = 32, HKV = 8, SQ = 32, SK = 2048, D = 128;
constexpr int NSPLIT = 4, KPS = SK / NSPLIT;
constexpr int TK = 64;
constexpr int NT = KPS / TK;
constexpr int NSLOT = 4;
constexpr int HALF = TK * D * 2;
constexpr int SLOT = 2 * HALF;
constexpr int LDS_Q = NSLOT * SLOT;
constexpr int LDS_BYTES = NSLOT * SLOT + 32768;
constexpr int LDS_STATS = 8 * 8192;
constexpr float CEXP = 0.088388347648318447f * 1.4426950408889634f;
constexpr float THR = 10.f;
constexpr size_t WS_PO = 0;
constexpr size_t WS_ST = (size_t)64 * 4 * 4 * 32 * 128 * 2;

#define STPOL 2
#define LDS_AS __attribute__((address_space(3)))
#define SB() __builtin_amdgcn_sched_barrier(0)

__device__ __forceinline__ unsigned pk2(float a, float b) {
  f32x2 v = {a, b};
  bf16x2_t r = __builtin_convertvector(v, bf16x2_t);
  return __builtin_bit_cast(unsigned, r);
}
__device__ __forceinline__ float bflo(unsigned u) { return __uint_as_float(u << 16); }
__device__ __forceinline__ float bfhi(unsigned u) { return __uint_as_float(u & 0xffff0000u); }

__device__ __forceinline__ void dma2k(__amdgpu_buffer_rsrc_t rsrc, unsigned voff, unsigned soff, unsigned ldsaddr) {
  unsigned keep;
  asm volatile("s_mov_b32 %0, m0\n\ts_mov_b32 m0, %1\n\ts_nop 0\n\tbuffer_load_dwordx4 %2, %3, %4 offen nt lds\n\t"
               "s_add_u32 m0, m0, 0x380\n\ts_nop 0\n\tbuffer_load_dwordx4 %2, %3, %4 offen offset:128 nt lds\n\ts_mov_b32 m0, %0"
               : "=&s"(keep) : "s"(ldsaddr), "v"(voff), "s"(rsrc), "s"(soff) : "memory", "scc");
}

__device__ __forceinline__ void dma2k_def(__amdgpu_buffer_rsrc_t rsrc, unsigned voff, unsigned soff, unsigned ldsaddr) {
  unsigned keep;
  asm volatile("s_mov_b32 %0, m0\n\ts_mov_b32 m0, %1\n\ts_nop 0\n\tbuffer_load_dwordx4 %2, %3, %4 offen lds\n\t"
               "s_add_u32 m0, m0, 0x380\n\ts_nop 0\n\tbuffer_load_dwordx4 %2, %3, %4 offen offset:128 lds\n\ts_mov_b32 m0, %0"
               : "=&s"(keep) : "s"(ldsaddr), "v"(voff), "s"(rsrc), "s"(soff) : "memory", "scc");
}

#define ST(i)
template <int N> __device__ __forceinline__ void wait_bar() {
  if constexpr (N == 8) asm volatile("s_waitcnt vmcnt(16) lgkmcnt(0)\n\ts_barrier" ::: "memory");
  else if constexpr (N == 6) asm volatile("s_waitcnt vmcnt(12) lgkmcnt(0)\n\ts_barrier" ::: "memory");
  else if constexpr (N == 2) asm volatile("s_waitcnt vmcnt(4) lgkmcnt(0)\n\ts_barrier" ::: "memory");
  else if constexpr (N == 4) asm volatile("s_waitcnt vmcnt(8) lgkmcnt(0)\n\ts_barrier" ::: "memory");
  else asm volatile("s_waitcnt vmcnt(0) lgkmcnt(0)\n\ts_barrier" ::: "memory");
}

__global__ __launch_bounds__(512, 2) void gqa_split_kernel(const __hip_bfloat16* __restrict__ Qp, const __hip_bfloat16* __restrict__ Kp,
                                                           const __hip_bfloat16* __restrict__ Vp, char* __restrict__ ws, const __hip_bfloat16* __restrict__ Op) {
  __shared__ __attribute__((aligned(16))) char smem[LDS_BYTES];
  ST(0);
  const int tid = threadIdx.x, lane = tid & 63;
  const int wid = __builtin_amdgcn_readfirstlane(tid >> 6);
  const int g = wid & 3, kh = wid >> 2;
  const int r32 = lane & 31, hi = lane >> 5;
  const int xcd = blockIdx.x & 7, idx = blockIdx.x >> 3;
  const int grp = xcd * 8 + (idx >> 2), split = idx & 3;
  const int bid = grp * 4 + split;
  const size_t kvoff = ((size_t)grp * SK + (size_t)split * KPS) * D;
  __amdgpu_buffer_rsrc_t rK = __builtin_amdgcn_make_buffer_rsrc((void*)(Kp + kvoff), 0, KPS * D * 2, 0x00020000);
  __amdgpu_buffer_rsrc_t rV = __builtin_amdgcn_make_buffer_rsrc((void*)(Vp + kvoff), 0, KPS * D * 2, 0x00020000);
  const unsigned ldsbase = (unsigned)(uintptr_t)(LDS_AS char*)smem;

  const int dsub = lane >> 5, drr = (lane >> 2) & 7, dx = lane & 3;
  const unsigned dsrcA = (16 * g + drr) * 256 + (4 * dsub + (dx ^ ((drr >> 2) & 3))) * 16;
  const unsigned dsrcB = (16 * g + 8 + drr) * 256 + (4 * dsub + (dx ^ ((2 + (drr >> 2)) & 3))) * 16;
  auto issue = [&](int j) {
    if (kh == 0) {
      const unsigned lb = ldsbase + (j % NSLOT) * SLOT + g * 4096;
      if (j + 1 < NT) { dma2k(rK, dsrcA, (j + 1) * HALF, lb); dma2k(rK, dsrcB, (j + 1) * HALF, lb + 2048); }
      dma2k(rV, dsrcA, j * HALF, lb + HALF); dma2k(rV, dsrcB, j * HALF, lb + HALF + 2048);
    }
  };
  __amdgpu_buffer_rsrc_t rQ = __builtin_amdgcn_make_buffer_rsrc((void*)(Qp + (size_t)grp * 4 * SQ * D), 0, 4 * SQ * D * 2, 0x00020000);

  if (kh == 0) {
    const unsigned lb = ldsbase + (NSLOT - 1) * SLOT + g * 4096;
    dma2k(rK, dsrcA, 0, lb); dma2k(rK, dsrcB, 0, lb + 2048);
  } else {
    const unsigned lb = ldsbase + LDS_Q + g * 4096;
    dma2k_def(rQ, dsrcA, 0, lb); dma2k_def(rQ, dsrcB, 0, lb + 2048);
    dma2k_def(rQ, dsrcA, HALF, lb + HALF); dma2k_def(rQ, dsrcB, HALF, lb + HALF + 2048);
  }
  __builtin_amdgcn_s_barrier();
  issue(0);
  ST(1);

  const int krow = 32 * kh + r32;
  const int ka0 = 2048 * (krow >> 3) + 64 * (r32 & 7) + 16 * ((0 + hi) ^ ((r32 >> 2) & 3));
  const int ka1 = 2048 * (krow >> 3) + 64 * (r32 & 7) + 16 * ((2 + hi) ^ ((r32 >> 2) & 3));
  const int gq = lane >> 4, i16 = lane & 15, qp = i16 >> 2, pp = i16 & 3;
  const int vh = gq >> 1, vc = 2 * (gq & 1) + (pp >> 1);
  const int va0 = HALF + 8192 * kh + 64 * (4 * vh + qp) + 16 * (vc ^ vh) + 8 * (pp & 1);
  const int va1 = HALF + 8192 * kh + 2048 + 64 * (4 * vh + qp) + 16 * (vc ^ (2 + vh)) + 8 * (pp & 1);

  f32x16 o[4];
#pragma unroll
  for (int i = 0; i < 4; ++i)
#pragma unroll
    for (int r = 0; r < 16; ++r) o[i][r] = 0.f;
  float m = -__builtin_inff(), l = 0.f;
  f32x16 negm;
#pragma unroll
  for (int r = 0; r < 16; ++r) negm[r] = 0.f;
  float mref = 0.f;

  bf16x8 qf[8];
  bf16x8 kf[8];
  bf16x8 vf[4][2];
  auto ldk = [&](int t) {
    const char* sp = smem + ((t + NSLOT - 1) % NSLOT) * SLOT;
#pragma unroll
    for (int s = 0; s < 8; ++s) kf[s] = *(const bf16x8*)(sp + ((s & 1) ? ka1 : ka0) + 512 * (s >> 1));
  };
  auto ldv = [&](int t) {
    const char* sp = smem + (t % NSLOT) * SLOT;
#pragma unroll
    for (int s = 0; s < 2; ++s)
#pragma unroll
      for (int db = 0; db < 4; ++db) {
        s16x4 v0 = __builtin_amdgcn_ds_read_tr16_b64_v4i16((LDS_AS s16x4*)(sp + va0 + 4096 * s + 512 * db));
        s16x4 v1 = __builtin_amdgcn_ds_read_tr16_b64_v4i16((LDS_AS s16x4*)(sp + va1 + 4096 * s + 512 * db));
        vf[db][s] = __builtin_shufflevector(v0, v1, 0, 1, 2, 3, 4, 5, 6, 7);
      }
  };
  auto qk = [&]() {
    f32x16 sc = __builtin_amdgcn_mfma_f32_32x32x16_bf16(kf[0], qf[0], negm, 0, 0, 0);
#pragma unroll
    for (int s = 1; s < 8; ++s) sc = __builtin_amdgcn_mfma_f32_32x32x16_bf16(kf[s], qf[s], sc, 0, 0, 0);
    return sc;
  };
  auto rowmax = [&](const f32x16& sc) {
    float mx = sc[0];
#pragma unroll
    for (int r = 1; r < 16; ++r) mx = fmaxf(mx, sc[r]);
    auto rr = __builtin_amdgcn_permlane32_swap(__float_as_uint(mx), __float_as_uint(mx), false, false);
    return fmaxf(__uint_as_float(rr[0]), __uint_as_float(rr[1]));
  };
  auto fixmax = [&](f32x16& sc, float mx, float mS) {
    const bool slow = !(mx <= THR) || (mS != m);
    if (__builtin_expect(__builtin_amdgcn_ballot_w64(slow) != 0, 0)) {
      const float mn = fmaxf(m, mx + mS);
      const float alpha = __builtin_amdgcn_exp2f(m - mn);
      const float dlt = mS - mn;
      m = mn; mref = mn;
      l *= alpha;
#pragma unroll
      for (int r = 0; r < 16; ++r) { sc[r] += dlt; negm[r] = -mn; }
#pragma unroll
      for (int db = 0; db < 4; ++db)
#pragma unroll
        for (int r = 0; r < 16; ++r) o[db][r] *= alpha;
    }
  };
  bf16x8 pf[2];
  auto expo = [&](f32x16& sc) {
    float ps = 0.f;
#pragma unroll
    for (int r = 0; r < 16; ++r) { sc[r] = __builtin_amdgcn_exp2f(sc[r]); ps += sc[r]; }
    l += ps;
#pragma unroll
    for (int s = 0; s < 2; ++s) {
      u32x4 w = {pk2(sc[8 * s + 0], sc[8 * s + 1]), pk2(sc[8 * s + 2], sc[8 * s + 3]), pk2(sc[8 * s + 4], sc[8 * s + 5]), pk2(sc[8 * s + 6], sc[8 * s + 7])};
      pf[s] = __builtin_bit_cast(bf16x8, w);
    }
  };
  auto pv = [&]() {
#pragma unroll
    for (int s = 0; s < 2; ++s)
#pragma unroll
      for (int db = 0; db < 4; ++db) o[db] = __builtin_amdgcn_mfma_f32_32x32x16_bf16(vf[db][s], pf[s], o[db], 0, 0, 0);
  };

  ST(2);
  if (kh) asm volatile("s_waitcnt vmcnt(0)" ::: "memory");
  wait_bar<4>();
  ST(3);
  {
    const int qrow = 32 * g + r32;
    const char* qp_ = smem + LDS_Q + 2048 * (qrow >> 3) + 64 * (r32 & 7);
#pragma unroll
    for (int s = 0; s < 8; ++s) {
      u32x4 raw = *(const u32x4*)(qp_ + 16 * ((2 * (s & 1) + hi) ^ ((r32 >> 2) & 3)) + 512 * (s >> 1));
      u32x4 w;
#pragma unroll
      for (int j = 0; j < 4; ++j) w[j] = pk2(bflo(raw[j]) * CEXP, bfhi(raw[j]) * CEXP);
      qf[s] = __builtin_bit_cast(bf16x8, w);
    }
  }
  ldk(0);
  f32x16 scur = qk();
  float mScur = mref;
  SB();
  issue(1);
#pragma unroll
  for (int t = 0; t < NT - 1; ++t) {
    ST(4 + 2 * t);
    if (t == 0) wait_bar<4>(); else if (t < NT - 3) wait_bar<8>(); else if (t == NT - 3) wait_bar<6>(); else wait_bar<2>();
    ST(5 + 2 * t);
    ldk(t + 1);
    SB();
    const float mx = rowmax(scur);
    if (t > 0) {
      pv();
#pragma unroll
      for (int i = 0; i < 8; ++i) {
        __builtin_amdgcn_sched_group_barrier(0x8, 1, 1);
        __builtin_amdgcn_sched_group_barrier(0x402, 2, 1);
      }
    }
    SB();
    ldv(t);
    SB();
    fixmax(scur, mx, mScur);
    SB();
    const float mSn = mref;
    f32x16 snext = qk();
    expo(scur);
#pragma unroll
    for (int i = 0; i < 8; ++i) {
      __builtin_amdgcn_sched_group_barrier(0x8, 1, 0);
      __builtin_amdgcn_sched_group_barrier(0x402, 5, 0);
    }
    SB();
    scur = snext; mScur = mSn;
    if (t == 0) issue(2);
    if (t + 3 < NT) issue(t + 3);
  }
  {
    const float mx = rowmax(scur);
    pv();
#pragma unroll
    for (int i = 0; i < 8; ++i) {
      __builtin_amdgcn_sched_group_barrier(0x8, 1, 1);
      __builtin_amdgcn_sched_group_barrier(0x402, 2, 1);
    }
    SB();
    fixmax(scur, mx, mScur);
    expo(scur);
  }
  {
    auto rr = __builtin_amdgcn_permlane32_swap(__float_as_uint(l), __float_as_uint(l), false, false);
    l = __uint_as_float(rr[0]) + __uint_as_float(rr[1]);
  }
  float* stats = (float*)(smem + LDS_Q);
  if (hi == 0) { stats[wid * 64 + r32] = m; stats[wid * 64 + 32 + r32] = l; }
  ST(18);
  wait_bar<0>();
  ldv(NT - 1);
  const int pw = wid ^ 4;
  const float m2 = stats[pw * 64 + r32], l2 = stats[pw * 64 + 32 + r32];
  pv();
  const float M = fmaxf(m, m2);
  const float e1 = __builtin_amdgcn_exp2f(m - M), e2 = __builtin_amdgcn_exp2f(m2 - M);
  const float L = l * e1 + l2 * e2;
  const float f1 = e1 / L;
  ST(19);

  char* xb = smem + wid * 8192;
#pragma unroll
  for (int j = 0; j < 2; ++j)
#pragma unroll
    for (int q4 = 0; q4 < 4; ++q4) {
      const f32x16& src = kh ? o[j] : o[2 + j];
      f32x4 v = {src[4 * q4] * f1, src[4 * q4 + 1] * f1, src[4 * q4 + 2] * f1, src[4 * q4 + 3] * f1};
      *(f32x4*)(xb + (j * 4 + q4) * 1024 + lane * 16) = v;
    }
  if (kh == 0 && hi == 0) {
    float* st = (float*)(ws + WS_ST) + (size_t)(bid * 4 + g) * 64;
    st[r32] = M; st[32 + r32] = L;
  }
  __syncthreads();
  const char* pb = smem + pw * 8192;
  __amdgpu_buffer_rsrc_t rW = __builtin_amdgcn_make_buffer_rsrc((void*)ws, 0, 16 << 20, 0x00020000);
  char* po = ws + WS_PO + ((size_t)(bid * 4 + g)) * (32 * 128 * 2);
#pragma unroll
  for (int j = 0; j < 2; ++j) {
    const f32x16& own = kh ? o[2 + j] : o[j];
#pragma unroll
    for (int pr = 0; pr < 2; ++pr) {
      f32x4 a = *(const f32x4*)(pb + (j * 4 + 2 * pr) * 1024 + lane * 16);
      f32x4 b = *(const f32x4*)(pb + (j * 4 + 2 * pr + 1) * 1024 + lane * 16);
      u32x4 w = {pk2(own[8 * pr + 0] * f1 + a[0], own[8 * pr + 1] * f1 + a[1]), pk2(own[8 * pr + 2] * f1 + a[2], own[8 * pr + 3] * f1 + a[3]),
                 pk2(own[8 * pr + 4] * f1 + b[0], own[8 * pr + 5] * f1 + b[1]), pk2(own[8 * pr + 6] * f1 + b[2], own[8 * pr + 7] * f1 + b[3])};
      const int db = 2 * kh + j;
      __builtin_amdgcn_raw_buffer_store_b128(w, rW, (unsigned)((bid * 4 + g) * 8192 + (db * 2 + pr) * 1024 + lane * 16), 0, 16);
    }
  }
}

__global__ __launch_bounds__(256) void gqa_combine_kernel(const char* __restrict__ ws, __hip_bfloat16* __restrict__ Op) {
  const int tid = threadIdx.x, lane = tid & 63;
  const int cx = blockIdx.x & 7, ci = blockIdx.x >> 3;
  const int wv = ((cx * 8 + (ci >> 3)) * 8 + (ci & 7)) * 4 + (tid >> 6);
  const int part = wv & 7, gh = wv >> 3;
  const int grp = gh >> 2, g = gh & 3;
  const int r32 = lane & 31, hi = lane >> 5;
  u32x4 p[4]; float ms[4], ls[4];
#pragma unroll
  for (int s = 0; s < 4; ++s) {
    const size_t u = (size_t)((grp * 4 + s) * 4 + g);
    p[s] = *(const u32x4*)(ws + WS_PO + u * (32 * 128 * 2) + part * 1024 + lane * 16);
    const float* st = (const float*)(ws + WS_ST) + u * 64;
    ms[s] = st[r32]; ls[s] = st[32 + r32];
  }
  const float M = fmaxf(fmaxf(ms[0], ms[1]), fmaxf(ms[2], ms[3]));
  float w[4], W = 0.f;
#pragma unroll
  for (int s = 0; s < 4; ++s) { w[s] = ls[s] * __builtin_amdgcn_exp2f(ms[s] - M); W += w[s]; }
  const float rW = 1.f / W;
  float acc[8];
#pragma unroll
  for (int j = 0; j < 8; ++j) acc[j] = 0.f;
#pragma unroll
  for (int s = 0; s < 4; ++s) {
    const float ww = w[s] * rW;
#pragma unroll
    for (int j = 0; j < 4; ++j) { acc[2 * j] += ww * bflo(p[s][j]); acc[2 * j + 1] += ww * bfhi(p[s][j]); }
  }
  const int db = part >> 1, pr = part & 1;
  __hip_bfloat16* orow = Op + ((size_t)gh * SQ + r32) * D + db * 32 + pr * 16 + 4 * hi;
  u32x2 a = {pk2(acc[0], acc[1]), pk2(acc[2], acc[3])};
  u32x2 b = {pk2(acc[4], acc[5]), pk2(acc[6], acc[7])};
  *(u32x2*)(orow) = a;
  *(u32x2*)(orow + 8) = b;
}

extern "C" void kernel_launch(void* const* d_in, const int* in_sizes, int n_in, void* d_out, int out_size, void* d_ws, size_t ws_size,
                              hipStream_t stream) {
  const __hip_bfloat16* q = (const __hip_bfloat16*)d_in[0];
  const __hip_bfloat16* k = (const __hip_bfloat16*)d_in[1];
  const __hip_bfloat16* v = (const __hip_bfloat16*)d_in[2];
  __hip_bfloat16* o = (__hip_bfloat16*)d_out;
  hipLaunchKernelGGL(gqa_split_kernel, dim3(256), dim3(512), 0, stream, q, k, v, (char*)d_ws, o);
  hipLaunchKernelGGL(gqa_combine_kernel, dim3(512), dim3(256), 0, stream, (const char*)d_ws, o);
}
```

```cpp
#include <hip/hip_runtime.h>
#include <hip/hip_bf16.h>
#include <stdint.h>

typedef __attribute__((ext_vector_type(8))) short bf16x8;
typedef __attribute__((ext_vector_type(4))) short s16x4;
typedef __attribute__((ext_vector_type(16))) float f32x16;
typedef __attribute__((ext_vector_type(4))) float f32x4;
typedef __attribute__((ext_vector_type(2))) float f32x2;
typedef __attribute__((ext_vector_type(2))) __bf16 bf16x2_t;
typedef __attribute__((ext_vector_type(4))) unsigned u32x4;
typedef __attribute__((ext_vector_type(2))) unsigned u32x2;

constexpr int Bn = 8, HQ = 32, HKV = 8, SQ = 32, SK = 2048, D = 128;
constexpr int NSPLIT = 4, KPS = SK / NSPLIT;
constexpr int TK = 64;
constexpr int NT = KPS / TK;
constexpr int NSLOT = 4;
constexpr int HALF = TK * D * 2;
constexpr int SLOT = 2 * HALF;
constexpr int LDS_Q = NSLOT * SLOT;
constexpr int LDS_BYTES = NSLOT * SLOT + 32768;
constexpr int LDS_STATS = 8 * 8192;
constexpr float CEXP = 0.088388347648318447f * 1.4426950408889634f;
constexpr float THR = 10.f;
constexpr unsigned WS_PO = 0;
constexpr unsigned WS_ST = 64 * 4 * 4 * 8192;
constexpr unsigned WS_FL = WS_ST + 64 * 4 * 4 * 256;
constexpr unsigned WS_END = WS_FL + 64 * 16 * 64;
constexpr int LDS_OWN = 65536;

#define CODEPF 2
#define STPOL 2
extern "C" __device__ unsigned long long gqa_dispatch_id() __asm("llvm.amdgcn.dispatch.id");
#define LDS_AS __attribute__((address_space(3)))
#define SB() __builtin_amdgcn_sched_barrier(0)

__device__ __forceinline__ unsigned pk2(float a, float b) {
  f32x2 v = {a, b};
  bf16x2_t r = __builtin_convertvector(v, bf16x2_t);
  return __builtin_bit_cast(unsigned, r);
}
__device__ __forceinline__ float bflo(unsigned u) { return __uint_as_float(u << 16); }
__device__ __forceinline__ float bfhi(unsigned u) { return __uint_as_float(u & 0xffff0000u); }

__device__ __forceinline__ void dma2k(__amdgpu_buffer_rsrc_t rsrc, unsigned voff, unsigned soff, unsigned ldsaddr) {
  unsigned keep;
  asm volatile("s_mov_b32 %0, m0\n\ts_mov_b32 m0, %1\n\ts_nop 0\n\tbuffer_load_dwordx4 %2, %3, %4 offen nt lds\n\t"
               "s_add_u32 m0, m0, 0x380\n\ts_nop 0\n\tbuffer_load_dwordx4 %2, %3, %4 offen offset:128 nt lds\n\ts_mov_b32 m0, %0"
               : "=&s"(keep) : "s"(ldsaddr), "v"(voff), "s"(rsrc), "s"(soff) : "memory", "scc");
}

__device__ __forceinline__ void dma2k_def(__amdgpu_buffer_rsrc_t rsrc, unsigned voff, unsigned soff, unsigned ldsaddr) {
  unsigned keep;
  asm volatile("s_mov_b32 %0, m0\n\ts_mov_b32 m0, %1\n\ts_nop 0\n\tbuffer_load_dwordx4 %2, %3, %4 offen lds\n\t"
               "s_add_u32 m0, m0, 0x380\n\ts_nop 0\n\tbuffer_load_dwordx4 %2, %3, %4 offen offset:128 lds\n\ts_mov_b32 m0, %0"
               : "=&s"(keep) : "s"(ldsaddr), "v"(voff), "s"(rsrc), "s"(soff) : "memory", "scc");
}

#define ST(i)
template <int N> __device__ __forceinline__ void wait_bar() {
  if constexpr (N == 8) asm volatile("s_waitcnt vmcnt(16) lgkmcnt(0)\n\ts_barrier" ::: "memory");
  else if constexpr (N == 6) asm volatile("s_waitcnt vmcnt(12) lgkmcnt(0)\n\ts_barrier" ::: "memory");
  else if constexpr (N == 2) asm volatile("s_waitcnt vmcnt(4) lgkmcnt(0)\n\ts_barrier" ::: "memory");
  else if constexpr (N == 4) asm volatile("s_waitcnt vmcnt(8) lgkmcnt(0)\n\ts_barrier" ::: "memory");
  else asm volatile("s_waitcnt vmcnt(0) lgkmcnt(0)\n\ts_barrier" ::: "memory");
}

__global__ __launch_bounds__(512, 2) void gqa_split_kernel(const __hip_bfloat16* __restrict__ Qp, const __hip_bfloat16* __restrict__ Kp,
                                                           const __hip_bfloat16* __restrict__ Vp, char* __restrict__ ws, __hip_bfloat16* __restrict__ Op) {
  __shared__ __attribute__((aligned(16))) char smem[LDS_BYTES];
  ST(0);
  const int tid = threadIdx.x, lane = tid & 63;
  const int wid = __builtin_amdgcn_readfirstlane(tid >> 6);
  const int g = wid & 3, kh = wid >> 2;
  const int r32 = lane & 31, hi = lane >> 5;
  const int xcd = blockIdx.x & 7, idx = blockIdx.x >> 3;
  const int grp = xcd * 8 + (idx >> 2), split = idx & 3;
  const int bid = grp * 4 + split;
  const size_t kvoff = ((size_t)grp * SK + (size_t)split * KPS) * D;
  __amdgpu_buffer_rsrc_t rK = __builtin_amdgcn_make_buffer_rsrc((void*)(Kp + kvoff), 0, KPS * D * 2, 0x00020000);
  __amdgpu_buffer_rsrc_t rV = __builtin_amdgcn_make_buffer_rsrc((void*)(Vp + kvoff), 0, KPS * D * 2, 0x00020000);
  const unsigned ldsbase = (unsigned)(uintptr_t)(LDS_AS char*)smem;

  const int dsub = lane >> 5, drr = (lane >> 2) & 7, dx = lane & 3;
  const unsigned dsrcA = (16 * g + drr) * 256 + (4 * dsub + (dx ^ ((drr >> 2) & 3))) * 16;
  const unsigned dsrcB = (16 * g + 8 + drr) * 256 + (4 * dsub + (dx ^ ((2 + (drr >> 2)) & 3))) * 16;
  auto issue = [&](int j) {
    if (kh == 0) {
      const unsigned lb = ldsbase + (j % NSLOT) * SLOT + g * 4096;
      if (j + 1 < NT) { dma2k(rK, dsrcA, (j + 1) * HALF, lb); dma2k(rK, dsrcB, (j + 1) * HALF, lb + 2048); }
      dma2k(rV, dsrcA, j * HALF, lb + HALF); dma2k(rV, dsrcB, j * HALF, lb + HALF + 2048);
    }
  };
  __amdgpu_buffer_rsrc_t rQ = __builtin_amdgcn_make_buffer_rsrc((void*)(Qp + (size_t)grp * 4 * SQ * D), 0, 4 * SQ * D * 2, 0x00020000);

  if (kh == 0) {
    const unsigned lb = ldsbase + (NSLOT - 1) * SLOT + g * 4096;
    dma2k(rK, dsrcA, 0, lb); dma2k(rK, dsrcB, 0, lb + 2048);
  } else {
    const unsigned lb = ldsbase + LDS_Q + g * 4096;
    dma2k_def(rQ, dsrcA, 0, lb); dma2k_def(rQ, dsrcB, 0, lb + 2048);
    dma2k_def(rQ, dsrcA, HALF, lb + HALF); dma2k_def(rQ, dsrcB, HALF, lb + HALF + 2048);
  }
  u32x4 pfd0, pfd1, pfd2, pfd3;
  if (kh) {
    unsigned pclo, pchi;
    asm volatile("s_getpc_b64 s[40:41]\n\ts_add_u32 s40, s40, _Z16gqa_split_kernelPK14__hip_bfloat16S1_S1_PcPS_@rel32@lo+4\n\ts_addc_u32 s41, s41, _Z16gqa_split_kernelPK14__hip_bfloat16S1_S1_PcPS_@rel32@hi+12\n\ts_mov_b32 %0, s40\n\ts_mov_b32 %1, s41"
                 : "=s"(pclo), "=s"(pchi) :: "scc", "s40", "s41");
    const unsigned long long pc = ((unsigned long long)pchi << 32) | pclo;
    unsigned pelo, pehi;
    asm volatile("s_getpc_b64 s[40:41]\n\ts_add_u32 s40, s40, _Z18gqa_combine_kernelPKcP14__hip_bfloat16@rel32@lo+4\n\ts_addc_u32 s41, s41, _Z18gqa_combine_kernelPKcP14__hip_bfloat16@rel32@hi+12\n\ts_mov_b32 %0, s40\n\ts_mov_b32 %1, s41"
                 : "=s"(pelo), "=s"(pehi) :: "scc", "s40", "s41");
    const unsigned long long pe = (((unsigned long long)pehi << 32) | pelo) - 16;
    const unsigned long long a0 = pc + g * 4096 + lane * 16;
    const char* c0 = (const char*)(a0 < pe ? a0 : pe);
    const char* c1 = (const char*)(a0 + 1024 < pe ? a0 + 1024 : pe);
    const char* c2 = (const char*)(a0 + 2048 < pe ? a0 + 2048 : pe);
    const char* c3 = (const char*)(a0 + 3072 < pe ? a0 + 3072 : pe);
    asm volatile("global_load_dwordx4 %0, %4, off\n\tglobal_load_dwordx4 %1, %5, off\n\tglobal_load_dwordx4 %2, %6, off\n\tglobal_load_dwordx4 %3, %7, off"
                 : "=&v"(pfd0), "=&v"(pfd1), "=&v"(pfd2), "=&v"(pfd3) : "v"(c0), "v"(c1), "v"(c2), "v"(c3) : "memory");
  }
  __builtin_amdgcn_s_barrier();
  issue(0);
  ST(1);

  const int krow = 32 * kh + r32;
  const int ka0 = 2048 * (krow >> 3) + 64 * (r32 & 7) + 16 * ((0 + hi) ^ ((r32 >> 2) & 3));
  const int ka1 = 2048 * (krow >> 3) + 64 * (r32 & 7) + 16 * ((2 + hi) ^ ((r32 >> 2) & 3));
  const int gq = lane >> 4, i16 = lane & 15, qp = i16 >> 2, pp = i16 & 3;
  const int vh = gq >> 1, vc = 2 * (gq & 1) + (pp >> 1);
  const int va0 = HALF + 8192 * kh + 64 * (4 * vh + qp) + 16 * (vc ^ vh) + 8 * (pp & 1);
  const int va1 = HALF + 8192 * kh + 2048 + 64 * (4 * vh + qp) + 16 * (vc ^ (2 + vh)) + 8 * (pp & 1);

  f32x16 o[4];
#pragma unroll
  for (int i = 0; i < 4; ++i)
#pragma unroll
    for (int r = 0; r < 16; ++r) o[i][r] = 0.f;
  float m = -__builtin_inff(), l = 0.f;
  f32x16 negm;
#pragma unroll
  for (int r = 0; r < 16; ++r) negm[r] = 0.f;
  float mref = 0.f;

  bf16x8 qf[8];
  bf16x8 kf[8];
  bf16x8 vf[4][2];
  auto ldk = [&](int t) {
    const char* sp = smem + ((t + NSLOT - 1) % NSLOT) * SLOT;
#pragma unroll
    for (int s = 0; s < 8; ++s) kf[s] = *(const bf16x8*)(sp + ((s & 1) ? ka1 : ka0) + 512 * (s >> 1));
  };
  auto ldv = [&](int t) {
    const char* sp = smem + (t % NSLOT) * SLOT;
#pragma unroll
    for (int s = 0; s < 2; ++s)
#pragma unroll
      for (int db = 0; db < 4; ++db) {
        s16x4 v0 = __builtin_amdgcn_ds_read_tr16_b64_v4i16((LDS_AS s16x4*)(sp + va0 + 4096 * s + 512 * db));
        s16x4 v1 = __builtin_amdgcn_ds_read_tr16_b64_v4i16((LDS_AS s16x4*)(sp + va1 + 4096 * s + 512 * db));
        vf[db][s] = __builtin_shufflevector(v0, v1, 0, 1, 2, 3, 4, 5, 6, 7);
      }
  };
  auto qk = [&]() {
    f32x16 sc = __builtin_amdgcn_mfma_f32_32x32x16_bf16(kf[0], qf[0], negm, 0, 0, 0);
#pragma unroll
    for (int s = 1; s < 8; ++s) sc = __builtin_amdgcn_mfma_f32_32x32x16_bf16(kf[s], qf[s], sc, 0, 0, 0);
    return sc;
  };
  auto rowmax = [&](const f32x16& sc) {
    float mx = sc[0];
#pragma unroll
    for (int r = 1; r < 16; ++r) mx = fmaxf(mx, sc[r]);
    auto rr = __builtin_amdgcn_permlane32_swap(__float_as_uint(mx), __float_as_uint(mx), false, false);
    return fmaxf(__uint_as_float(rr[0]), __uint_as_float(rr[1]));
  };
  auto fixmax = [&](f32x16& sc, float mx, float mS) {
    const bool slow = !(mx <= THR) || (mS != m);
    if (__builtin_expect(__builtin_amdgcn_ballot_w64(slow) != 0, 0)) {
      const float mn = fmaxf(m, mx + mS);
      const float alpha = __builtin_amdgcn_exp2f(m - mn);
      const float dlt = mS - mn;
      m = mn; mref = mn;
      l *= alpha;
#pragma unroll
      for (int r = 0; r < 16; ++r) { sc[r] += dlt; negm[r] = -mn; }
#pragma unroll
      for (int db = 0; db < 4; ++db)
#pragma unroll
        for (int r = 0; r < 16; ++r) o[db][r] *= alpha;
    }
  };
  bf16x8 pf[2];
  auto expo = [&](f32x16& sc) {
    float ps = 0.f;
#pragma unroll
    for (int r = 0; r < 16; ++r) { sc[r] = __builtin_amdgcn_exp2f(sc[r]); ps += sc[r]; }
    l += ps;
#pragma unroll
    for (int s = 0; s < 2; ++s) {
      u32x4 w = {pk2(sc[8 * s + 0], sc[8 * s + 1]), pk2(sc[8 * s + 2], sc[8 * s + 3]), pk2(sc[8 * s + 4], sc[8 * s + 5]), pk2(sc[8 * s + 6], sc[8 * s + 7])};
      pf[s] = __builtin_bit_cast(bf16x8, w);
    }
  };
  auto pv = [&]() {
#pragma unroll
    for (int s = 0; s < 2; ++s)
#pragma unroll
      for (int db = 0; db < 4; ++db) o[db] = __builtin_amdgcn_mfma_f32_32x32x16_bf16(vf[db][s], pf[s], o[db], 0, 0, 0);
  };

  ST(2);
  if (kh) asm volatile("s_waitcnt vmcnt(0)" : "+v"(pfd0), "+v"(pfd1), "+v"(pfd2), "+v"(pfd3) :: "memory");
  wait_bar<4>();
  ST(3);
  {
    const int qrow = 32 * g + r32;
    const char* qp_ = smem + LDS_Q + 2048 * (qrow >> 3) + 64 * (r32 & 7);
#pragma unroll
    for (int s = 0; s < 8; ++s) {
      u32x4 raw = *(const u32x4*)(qp_ + 16 * ((2 * (s & 1) + hi) ^ ((r32 >> 2) & 3)) + 512 * (s >> 1));
      u32x4 w;
#pragma unroll
      for (int j = 0; j < 4; ++j) w[j] = pk2(bflo(raw[j]) * CEXP, bfhi(raw[j]) * CEXP);
      qf[s] = __builtin_bit_cast(bf16x8, w);
    }
  }
  ldk(0);
  f32x16 scur = qk();
  float mScur = mref;
  SB();
  issue(1);
#pragma unroll
  for (int t = 0; t < NT - 1; ++t) {
    ST(4 + 2 * t);
    if (t == 0) wait_bar<4>(); else if (t < NT - 3) wait_bar<8>(); else if (t == NT - 3) wait_bar<6>(); else wait_bar<2>();
    ST(5 + 2 * t);
    ldk(t + 1);
    SB();
    const float mx = rowmax(scur);
    if (t > 0) {
      pv();
#pragma unroll
      for (int i = 0; i < 8; ++i) {
        __builtin_amdgcn_sched_group_barrier(0x8, 1, 1);
        __builtin_amdgcn_sched_group_barrier(0x402, 2, 1);
      }
    }
    SB();
    ldv(t);
    SB();
    fixmax(scur, mx, mScur);
    SB();
    const float mSn = mref;
    f32x16 snext = qk();
    expo(scur);
#pragma unroll
    for (int i = 0; i < 8; ++i) {
      __builtin_amdgcn_sched_group_barrier(0x8, 1, 0);
      __builtin_amdgcn_sched_group_barrier(0x402, 5, 0);
    }
    SB();
    scur = snext; mScur = mSn;
    if (t == 0) issue(2);
    if (t + 3 < NT) issue(t + 3);
  }
  ST(18);
  wait_bar<0>();
  bf16x8 vg[4][2];
  {
    const char* sp = smem + ((NT - 1) % NSLOT) * SLOT;
#pragma unroll
    for (int s = 0; s < 2; ++s)
#pragma unroll
      for (int db = 0; db < 4; ++db) {
        s16x4 v0 = __builtin_amdgcn_ds_read_tr16_b64_v4i16((LDS_AS s16x4*)(sp + va0 + 4096 * s + 512 * db));
        s16x4 v1 = __builtin_amdgcn_ds_read_tr16_b64_v4i16((LDS_AS s16x4*)(sp + va1 + 4096 * s + 512 * db));
        vg[db][s] = __builtin_shufflevector(v0, v1, 0, 1, 2, 3, 4, 5, 6, 7);
      }
  }
  bf16x8 pg[2];
  auto expo2 = [&](f32x16& sc) {
    float ps = 0.f;
#pragma unroll
    for (int r = 0; r < 16; ++r) { sc[r] = __builtin_amdgcn_exp2f(sc[r]); ps += sc[r]; }
    l += ps;
#pragma unroll
    for (int s = 0; s < 2; ++s) {
      u32x4 w = {pk2(sc[8 * s + 0], sc[8 * s + 1]), pk2(sc[8 * s + 2], sc[8 * s + 3]), pk2(sc[8 * s + 4], sc[8 * s + 5]), pk2(sc[8 * s + 6], sc[8 * s + 7])};
      pg[s] = __builtin_bit_cast(bf16x8, w);
    }
  };
  {
    const float mx = rowmax(scur);
    const bool slow = !(mx <= THR) || (mScur != m);
    if (__builtin_expect(__builtin_amdgcn_ballot_w64(slow) != 0, 0)) {
      pv();
      SB();
      fixmax(scur, mx, mScur);
      expo2(scur);
    } else {
      pv();
      expo2(scur);
#pragma unroll
      for (int i = 0; i < 8; ++i) {
        __builtin_amdgcn_sched_group_barrier(0x8, 1, 2);
        __builtin_amdgcn_sched_group_barrier(0x402, 5, 2);
      }
    }
  }
  SB();
  const int pw = wid ^ 4;
  char* xb = smem + wid * 4096;
  float* stats = (float*)(smem + LDS_Q);
  if (kh) {
#pragma unroll
    for (int s = 0; s < 2; ++s) { o[0] = __builtin_amdgcn_mfma_f32_32x32x16_bf16(vg[0][s], pg[s], o[0], 0, 0, 0); o[1] = __builtin_amdgcn_mfma_f32_32x32x16_bf16(vg[1][s], pg[s], o[1], 0, 0, 0); }
#pragma unroll
    for (int s = 0; s < 2; ++s) { o[2] = __builtin_amdgcn_mfma_f32_32x32x16_bf16(vg[2][s], pg[s], o[2], 0, 0, 0); o[3] = __builtin_amdgcn_mfma_f32_32x32x16_bf16(vg[3][s], pg[s], o[3], 0, 0, 0); }
  } else {
#pragma unroll
    for (int s = 0; s < 2; ++s) { o[2] = __builtin_amdgcn_mfma_f32_32x32x16_bf16(vg[2][s], pg[s], o[2], 0, 0, 0); o[3] = __builtin_amdgcn_mfma_f32_32x32x16_bf16(vg[3][s], pg[s], o[3], 0, 0, 0); }
#pragma unroll
    for (int s = 0; s < 2; ++s) { o[0] = __builtin_amdgcn_mfma_f32_32x32x16_bf16(vg[0][s], pg[s], o[0], 0, 0, 0); o[1] = __builtin_amdgcn_mfma_f32_32x32x16_bf16(vg[1][s], pg[s], o[1], 0, 0, 0); }
  }
  {
    auto rr = __builtin_amdgcn_permlane32_swap(__float_as_uint(l), __float_as_uint(l), false, false);
    l = __uint_as_float(rr[0]) + __uint_as_float(rr[1]);
  }
  if (hi == 0) { stats[wid * 64 + r32] = m; stats[wid * 64 + 32 + r32] = l; }
  ST(19);
#pragma unroll
  for (int j = 0; j < 2; ++j) {
    const f32x16& src = kh ? o[j] : o[2 + j];
#pragma unroll
    for (int pr = 0; pr < 2; ++pr) {
      u32x4 w = {pk2(src[8 * pr + 0], src[8 * pr + 1]), pk2(src[8 * pr + 2], src[8 * pr + 3]), pk2(src[8 * pr + 4], src[8 * pr + 5]), pk2(src[8 * pr + 6], src[8 * pr + 7])};
      *(u32x4*)(xb + (j * 2 + pr) * 1024 + lane * 16) = w;
    }
  }
  __syncthreads();
  const float m2 = stats[pw * 64 + r32], l2 = stats[pw * 64 + 32 + r32];
  const char* pb = smem + pw * 4096;
  u32x4 rc[2][2];
#pragma unroll
  for (int j = 0; j < 2; ++j)
#pragma unroll
    for (int pr = 0; pr < 2; ++pr) rc[j][pr] = *(const u32x4*)(pb + (j * 2 + pr) * 1024 + lane * 16);
  const float M = fmaxf(m, m2);
  const float e1 = __builtin_amdgcn_exp2f(m - M), e2 = __builtin_amdgcn_exp2f(m2 - M);
  const float L = l * e1 + l2 * e2;
  const float rL = 1.f / L;
  const float f1 = e1 * rL, f2 = e2 * rL;
  __amdgpu_buffer_rsrc_t rW = __builtin_amdgcn_make_buffer_rsrc((void*)ws, 0, WS_END, 0x00020000);
  const bool own_head = (g == split);
#pragma unroll
  for (int j = 0; j < 2; ++j) {
    const f32x16& own = kh ? o[2 + j] : o[j];
#pragma unroll
    for (int pr = 0; pr < 2; ++pr) {
      const u32x4 a = rc[j][pr];
      u32x4 w = {pk2(own[8 * pr + 0] * f1 + bflo(a[0]) * f2, own[8 * pr + 1] * f1 + bfhi(a[0]) * f2), pk2(own[8 * pr + 2] * f1 + bflo(a[1]) * f2, own[8 * pr + 3] * f1 + bfhi(a[1]) * f2),
                 pk2(own[8 * pr + 4] * f1 + bflo(a[2]) * f2, own[8 * pr + 5] * f1 + bfhi(a[2]) * f2), pk2(own[8 * pr + 6] * f1 + bflo(a[3]) * f2, own[8 * pr + 7] * f1 + bfhi(a[3]) * f2)};
      const int db = 2 * kh + j;
      if (own_head) *(u32x4*)(smem + LDS_OWN + (db * 2 + pr) * 1024 + lane * 16) = w;
      else __builtin_amdgcn_raw_buffer_store_b128(w, rW, WS_PO + (unsigned)((bid * 4 + g) * 8192 + (db * 2 + pr) * 1024 + lane * 16), 0, 16);
    }
  }
  if (kh == 0 && hi == 0) {
    if (own_head) { float* st = (float*)(smem + LDS_OWN + 8192); st[r32] = M; st[32 + r32] = L; }
    else {
      __builtin_amdgcn_raw_buffer_store_b32(__float_as_uint(M), rW, WS_ST + (unsigned)((bid * 4 + g) * 256 + r32 * 4), 0, 16);
      __builtin_amdgcn_raw_buffer_store_b32(__float_as_uint(L), rW, WS_ST + (unsigned)((bid * 4 + g) * 256 + 128 + r32 * 4), 0, 16);
    }
  }
  asm volatile("s_waitcnt vmcnt(0) lgkmcnt(0)" ::: "memory");
  __syncthreads();
  if (wid == 0) {
    const unsigned long long tag = (gqa_dispatch_id() + 1ull) * 0x9E3779B97F4A7C15ull ^ 0x5DEECE66DA3C1F27ull;
    if (lane < 3) {
      const int other = (split + 1 + lane) & 3;
      unsigned long long* fout = (unsigned long long*)(ws + WS_FL + ((grp * 4 + other) * 4 + split) * 64);
      unsigned long long* fin = (unsigned long long*)(ws + WS_FL + ((grp * 4 + split) * 4 + other) * 64);
      __hip_atomic_store(fout, tag, __ATOMIC_RELAXED, __HIP_MEMORY_SCOPE_AGENT);
      unsigned spins = 0;
      while (__hip_atomic_load(fin, __ATOMIC_RELAXED, __HIP_MEMORY_SCOPE_AGENT) != tag) {
        __builtin_amdgcn_s_sleep(1);
        if (++spins > (1u << 22)) break;
      }
      __hip_atomic_store(fin, 0ull, __ATOMIC_RELAXED, __HIP_MEMORY_SCOPE_AGENT);
    }
  }
  __syncthreads();
  {
    u32x4 p[4]; float ms[4], ls[4];
#pragma unroll
    for (int s = 0; s < 4; ++s) {
      const int sp_ = (split + s) & 3;
      if (s == 0) {
        p[0] = *(const u32x4*)(smem + LDS_OWN + wid * 1024 + lane * 16);
        const float* st = (const float*)(smem + LDS_OWN + 8192);
        ms[0] = st[r32]; ls[0] = st[32 + r32];
      } else {
        const unsigned u = (unsigned)((grp * 4 + sp_) * 4 + split);
        p[s] = __builtin_amdgcn_raw_buffer_load_b128(rW, WS_PO + u * 8192 + wid * 1024 + lane * 16, 0, 16);
        ms[s] = __uint_as_float(__builtin_amdgcn_raw_buffer_load_b32(rW, WS_ST + u * 256 + r32 * 4, 0, 16));
        ls[s] = __uint_as_float(__builtin_amdgcn_raw_buffer_load_b32(rW, WS_ST + u * 256 + 128 + r32 * 4, 0, 16));
      }
    }
    const float Mx = fmaxf(fmaxf(ms[0], ms[1]), fmaxf(ms[2], ms[3]));
    float wt[4], W = 0.f;
#pragma unroll
    for (int s = 0; s < 4; ++s) { wt[s] = ls[s] * __builtin_amdgcn_exp2f(ms[s] - Mx); W += wt[s]; }
    const float rWt = 1.f / W;
    float acc[8];
#pragma unroll
    for (int j = 0; j < 8; ++j) acc[j] = 0.f;
#pragma unroll
    for (int s = 0; s < 4; ++s) {
      const float ww = wt[s] * rWt;
#pragma unroll
      for (int j = 0; j < 4; ++j) { acc[2 * j] += ww * bflo(p[s][j]); acc[2 * j + 1] += ww * bfhi(p[s][j]); }
    }
    const int db = wid >> 1, pr = wid & 1;
    __hip_bfloat16* orow = Op + ((size_t)(grp * 4 + split) * SQ + r32) * D + db * 32 + pr * 16 + 4 * hi;
    u32x2 a = {pk2(acc[0], acc[1]), pk2(acc[2], acc[3])};
    u32x2 b = {pk2(acc[4], acc[5]), pk2(acc[6], acc[7])};
    *(u32x2*)(orow) = a;
    *(u32x2*)(orow + 8) = b;
  }
}

__global__ __launch_bounds__(64) void gqa_combine_kernel(const char* __restrict__ ws, __hip_bfloat16* __restrict__ Op) {
}

extern "C" void kernel_launch(void* const* d_in, const int* in_sizes, int n_in, void* d_out, int out_size, void* d_ws, size_t ws_size,
                              hipStream_t stream) {
  const __hip_bfloat16* q = (const __hip_bfloat16*)d_in[0];
  const __hip_bfloat16* k = (const __hip_bfloat16*)d_in[1];
  const __hip_bfloat16* v = (const __hip_bfloat16*)d_in[2];
  __hip_bfloat16* o = (__hip_bfloat16*)d_out;
  hipLaunchKernelGGL(gqa_split_kernel, dim3(256), dim3(512), 0, stream, q, k, v, (char*)d_ws, o);
}
```

```cpp
#include <hip/hip_runtime.h>
#include <hip/hip_bf16.h>
#include <stdint.h>

typedef __attribute__((ext_vector_type(8))) short bf16x8;
typedef __attribute__((ext_vector_type(4))) short s16x4;
typedef __attribute__((ext_vector_type(16))) float f32x16;
typedef __attribute__((ext_vector_type(4))) float f32x4;
typedef __attribute__((ext_vector_type(2))) float f32x2;
typedef __attribute__((ext_vector_type(2))) __bf16 bf16x2_t;
typedef __attribute__((ext_vector_type(4))) unsigned u32x4;
typedef __attribute__((ext_vector_type(2))) unsigned u32x2;

constexpr int Bn = 8, HQ = 32, HKV = 8, SQ = 32, SK = 2048, D = 128;
constexpr int NSPLIT = 4, KPS = SK / NSPLIT;
constexpr int TK = 64;
constexpr int NT = KPS / TK;
constexpr int NSLOT = 4;
constexpr int HALF = TK * D * 2;
constexpr int SLOT = 2 * HALF;
constexpr int LDS_Q = NSLOT * SLOT;
constexpr int LDS_BYTES = NSLOT * SLOT + 32768;
constexpr float CEXP = 0.088388347648318447f * 1.4426950408889634f;
constexpr float RCEXP = 1.f / CEXP;
constexpr float THR = 10.f;
constexpr unsigned WS_PO = 0;
constexpr unsigned WS_ST = 64 * 4 * 4 * 8192;
constexpr unsigned WS_FL = WS_ST + 64 * 4 * 4 * 256;
constexpr unsigned WS_END = WS_FL + 64 * 32 * 64;
constexpr int LDS_OWN = 65536;

extern "C" __device__ unsigned long long gqa_dispatch_id() __asm("llvm.amdgcn.dispatch.id");
#define LDS_AS __attribute__((address_space(3)))
#define SB() __builtin_amdgcn_sched_barrier(0)

__device__ __forceinline__ unsigned pk2(float a, float b) {
  f32x2 v = {a, b};
  bf16x2_t r = __builtin_convertvector(v, bf16x2_t);
  return __builtin_bit_cast(unsigned, r);
}
__device__ __forceinline__ float bflo(unsigned u) { return __uint_as_float(u << 16); }
__device__ __forceinline__ float bfhi(unsigned u) { return __uint_as_float(u & 0xffff0000u); }

__device__ __forceinline__ void dma2k(__amdgpu_buffer_rsrc_t rsrc, unsigned voff, unsigned soff, unsigned ldsaddr) {
  unsigned keep;
  asm volatile("s_mov_b32 %0, m0\n\ts_mov_b32 m0, %1\n\ts_nop 0\n\tbuffer_load_dwordx4 %2, %3, %4 offen nt lds\n\t"
               "s_add_u32 m0, m0, 0x380\n\ts_nop 0\n\tbuffer_load_dwordx4 %2, %3, %4 offen offset:128 nt lds\n\ts_mov_b32 m0, %0"
               : "=&s"(keep) : "s"(ldsaddr), "v"(voff), "s"(rsrc), "s"(soff) : "memory", "scc");
}

__device__ __forceinline__ void dma2k_def(__amdgpu_buffer_rsrc_t rsrc, unsigned voff, unsigned soff, unsigned ldsaddr) {
  unsigned keep;
  asm volatile("s_mov_b32 %0, m0\n\ts_mov_b32 m0, %1\n\ts_nop 0\n\tbuffer_load_dwordx4 %2, %3, %4 offen lds\n\t"
               "s_add_u32 m0, m0, 0x380\n\ts_nop 0\n\tbuffer_load_dwordx4 %2, %3, %4 offen offset:128 lds\n\ts_mov_b32 m0, %0"
               : "=&s"(keep) : "s"(ldsaddr), "v"(voff), "s"(rsrc), "s"(soff) : "memory", "scc");
}

template <int N> __device__ __forceinline__ void wait_bar() {
  if constexpr (N == 8) asm volatile("s_waitcnt vmcnt(16) lgkmcnt(0)\n\ts_barrier" ::: "memory");
  else if constexpr (N == 6) asm volatile("s_waitcnt vmcnt(12) lgkmcnt(0)\n\ts_barrier" ::: "memory");
  else if constexpr (N == 2) asm volatile("s_waitcnt vmcnt(4) lgkmcnt(0)\n\ts_barrier" ::: "memory");
  else if constexpr (N == 4) asm volatile("s_waitcnt vmcnt(8) lgkmcnt(0)\n\ts_barrier" ::: "memory");
  else asm volatile("s_waitcnt vmcnt(0) lgkmcnt(0)\n\ts_barrier" ::: "memory");
}

__global__ __launch_bounds__(512, 2) void gqa_split_kernel(const __hip_bfloat16* __restrict__ Qp, const __hip_bfloat16* __restrict__ Kp,
                                                           const __hip_bfloat16* __restrict__ Vp, char* __restrict__ ws, __hip_bfloat16* __restrict__ Op) {
  __shared__ __attribute__((aligned(16))) char smem[LDS_BYTES];
  const int tid = threadIdx.x, lane = tid & 63;
  const int wid = __builtin_amdgcn_readfirstlane(tid >> 6);
  const int g = wid & 3, kh = wid >> 2;
  const int r32 = lane & 31, hi = lane >> 5;
  const int xcd = blockIdx.x & 7, idx = blockIdx.x >> 3;
  const int grp = xcd * 8 + (idx >> 2), split = idx & 3;
  const int bid = grp * 4 + split;
  const size_t kvoff = ((size_t)grp * SK + (size_t)split * KPS) * D;
  __amdgpu_buffer_rsrc_t rK = __builtin_amdgcn_make_buffer_rsrc((void*)(Kp + kvoff), 0, KPS * D * 2, 0x00020000);
  __amdgpu_buffer_rsrc_t rV = __builtin_amdgcn_make_buffer_rsrc((void*)(Vp + kvoff), 0, KPS * D * 2, 0x00020000);
  const unsigned ldsbase = (unsigned)(uintptr_t)(LDS_AS char*)smem;

  const int dsub = lane >> 5, drr = (lane >> 2) & 7, dx = lane & 3;
  const unsigned dsrcA = (16 * g + drr) * 256 + (4 * dsub + (dx ^ ((drr >> 2) & 3))) * 16;
  const unsigned dsrcB = (16 * g + 8 + drr) * 256 + (4 * dsub + (dx ^ ((2 + (drr >> 2)) & 3))) * 16;
  auto issue = [&](int j) {
    if (kh == 0) {
      const unsigned lb = ldsbase + (j % NSLOT) * SLOT + g * 4096;
      if (j + 1 < NT) { dma2k(rK, dsrcA, (j + 1) * HALF, lb); dma2k(rK, dsrcB, (j + 1) * HALF, lb + 2048); }
      dma2k(rV, dsrcA, j * HALF, lb + HALF); dma2k(rV, dsrcB, j * HALF, lb + HALF + 2048);
    }
  };
  __amdgpu_buffer_rsrc_t rQ = __builtin_amdgcn_make_buffer_rsrc((void*)(Qp + (size_t)grp * 4 * SQ * D), 0, 4 * SQ * D * 2, 0x00020000);

  if (kh == 0) {
    const unsigned lb = ldsbase + (NSLOT - 1) * SLOT + g * 4096;
    dma2k(rK, dsrcA, 0, lb); dma2k(rK, dsrcB, 0, lb + 2048);
  } else {
    const unsigned lb = ldsbase + LDS_Q + g * 4096;
    dma2k_def(rQ, dsrcA, 0, lb); dma2k_def(rQ, dsrcB, 0, lb + 2048);
    dma2k_def(rQ, dsrcA, HALF, lb + HALF); dma2k_def(rQ, dsrcB, HALF, lb + HALF + 2048);
  }
  u32x4 pfd0, pfd1, pfd2, pfd3;
  if (kh) {
    unsigned pclo, pchi;
    asm volatile("s_getpc_b64 s[40:41]\n\ts_add_u32 s40, s40, _Z16gqa_split_kernelPK14__hip_bfloat16S1_S1_PcPS_@rel32@lo+4\n\ts_addc_u32 s41, s41, _Z16gqa_split_kernelPK14__hip_bfloat16S1_S1_PcPS_@rel32@hi+12\n\ts_mov_b32 %0, s40\n\ts_mov_b32 %1, s41"
                 : "=s"(pclo), "=s"(pchi) :: "scc", "s40", "s41");
    const unsigned long long pc = ((unsigned long long)pchi << 32) | pclo;
    unsigned pelo, pehi;
    asm volatile("s_getpc_b64 s[40:41]\n\ts_add_u32 s40, s40, _Z12gqa_code_endv@rel32@lo+4\n\ts_addc_u32 s41, s41, _Z12gqa_code_endv@rel32@hi+12\n\ts_mov_b32 %0, s40\n\ts_mov_b32 %1, s41"
                 : "=s"(pelo), "=s"(pehi) :: "scc", "s40", "s41");
    const unsigned long long pe = (((unsigned long long)pehi << 32) | pelo) - 16;
    const unsigned long long a0 = pc + g * 4096 + lane * 16;
    const char* c0 = (const char*)(a0 < pe ? a0 : pe);
    const char* c1 = (const char*)(a0 + 1024 < pe ? a0 + 1024 : pe);
    const char* c2 = (const char*)(a0 + 2048 < pe ? a0 + 2048 : pe);
    const char* c3 = (const char*)(a0 + 3072 < pe ? a0 + 3072 : pe);
    asm volatile("global_load_dwordx4 %0, %4, off\n\tglobal_load_dwordx4 %1, %5, off\n\tglobal_load_dwordx4 %2, %6, off\n\tglobal_load_dwordx4 %3, %7, off"
                 : "=&v"(pfd0), "=&v"(pfd1), "=&v"(pfd2), "=&v"(pfd3) : "v"(c0), "v"(c1), "v"(c2), "v"(c3) : "memory");
  }
  __builtin_amdgcn_s_barrier();
  issue(0);

  const int krow = 32 * kh + r32;
  const int ka0 = 2048 * (krow >> 3) + 64 * (r32 & 7) + 16 * ((0 + hi) ^ ((r32 >> 2) & 3));
  const int ka1 = 2048 * (krow >> 3) + 64 * (r32 & 7) + 16 * ((2 + hi) ^ ((r32 >> 2) & 3));
  const int gq = lane >> 4, i16 = lane & 15, qp = i16 >> 2, pp = i16 & 3;
  const int vh = gq >> 1, vc = 2 * (gq & 1) + (pp >> 1);
  const int va0 = HALF + 8192 * kh + 64 * (4 * vh + qp) + 16 * (vc ^ vh) + 8 * (pp & 1);
  const int va1 = HALF + 8192 * kh + 2048 + 64 * (4 * vh + qp) + 16 * (vc ^ (2 + vh)) + 8 * (pp & 1);

  f32x16 o[4];
#pragma unroll
  for (int i = 0; i < 4; ++i)
#pragma unroll
    for (int r = 0; r < 16; ++r) o[i][r] = 0.f;
  float m = -__builtin_inff(), l = 0.f;
  f32x16 negm;
#pragma unroll
  for (int r = 0; r < 16; ++r) negm[r] = 0.f;
  float mref = 0.f;

  bf16x8 qf[8];
  bf16x8 kf[8];
  bf16x8 vf[4][2];
  auto ldk = [&](int t) {
    const char* sp = smem + ((t + NSLOT - 1) % NSLOT) * SLOT;
#pragma unroll
    for (int s = 0; s < 8; ++s) kf[s] = *(const bf16x8*)(sp + ((s & 1) ? ka1 : ka0) + 512 * (s >> 1));
  };
  auto ldv = [&](int t) {
    const char* sp = smem + (t % NSLOT) * SLOT;
#pragma unroll
    for (int s = 0; s < 2; ++s)
#pragma unroll
      for (int db = 0; db < 4; ++db) {
        s16x4 v0 = __builtin_amdgcn_ds_read_tr16_b64_v4i16((LDS_AS s16x4*)(sp + va0 + 4096 * s + 512 * db));
        s16x4 v1 = __builtin_amdgcn_ds_read_tr16_b64_v4i16((LDS_AS s16x4*)(sp + va1 + 4096 * s + 512 * db));
        vf[db][s] = __builtin_shufflevector(v0, v1, 0, 1, 2, 3, 4, 5, 6, 7);
      }
  };
  auto qk = [&]() {
    f32x16 sc = __builtin_amdgcn_mfma_f32_32x32x16_bf16(kf[0], qf[0], negm, 0, 0, 0);
#pragma unroll
    for (int s = 1; s < 8; ++s) sc = __builtin_amdgcn_mfma_f32_32x32x16_bf16(kf[s], qf[s], sc, 0, 0, 0);
    return sc;
  };
  auto rowmax = [&](f32x16& sc) {
#pragma unroll
    for (int r = 0; r < 16; ++r) sc[r] *= CEXP;
    float mx = sc[0];
#pragma unroll
    for (int r = 1; r < 16; ++r) mx = fmaxf(mx, sc[r]);
    auto rr = __builtin_amdgcn_permlane32_swap(__float_as_uint(mx), __float_as_uint(mx), false, false);
    return fmaxf(__uint_as_float(rr[0]), __uint_as_float(rr[1]));
  };
  auto fixmax = [&](f32x16& sc, float mx, float mS) {
    const bool slow = !(mx <= THR) || (mS != m);
    if (__builtin_expect(__builtin_amdgcn_ballot_w64(slow) != 0, 0)) {
      const float mn = fmaxf(m, mS + mx * RCEXP);
      const float alpha = __builtin_amdgcn_exp2f((m - mn) * CEXP);
      const float dlt = (mS - mn) * CEXP;
      m = mn; mref = mn;
      l *= alpha;
#pragma unroll
      for (int r = 0; r < 16; ++r) { sc[r] += dlt; negm[r] = -mn; }
#pragma unroll
      for (int db = 0; db < 4; ++db)
#pragma unroll
        for (int r = 0; r < 16; ++r) o[db][r] *= alpha;
    }
  };
  bf16x8 pf[2];
  auto expo = [&](f32x16& sc) {
    float ps = 0.f;
#pragma unroll
    for (int r = 0; r < 16; ++r) { sc[r] = __builtin_amdgcn_exp2f(sc[r]); ps += sc[r]; }
    l += ps;
#pragma unroll
    for (int s = 0; s < 2; ++s) {
      u32x4 w = {pk2(sc[8 * s + 0], sc[8 * s + 1]), pk2(sc[8 * s + 2], sc[8 * s + 3]), pk2(sc[8 * s + 4], sc[8 * s + 5]), pk2(sc[8 * s + 6], sc[8 * s + 7])};
      pf[s] = __builtin_bit_cast(bf16x8, w);
    }
  };
  auto pv = [&]() {
#pragma unroll
    for (int s = 0; s < 2; ++s)
#pragma unroll
      for (int db = 0; db < 4; ++db) o[db] = __builtin_amdgcn_mfma_f32_32x32x16_bf16(vf[db][s], pf[s], o[db], 0, 0, 0);
  };
  if (kh) asm volatile("s_waitcnt vmcnt(0)" : "+v"(pfd0), "+v"(pfd1), "+v"(pfd2), "+v"(pfd3) :: "memory");
  wait_bar<4>();
  {
    const int qrow = 32 * g + r32;
    const char* qp_ = smem + LDS_Q + 2048 * (qrow >> 3) + 64 * (r32 & 7);
#pragma unroll
    for (int s = 0; s < 8; ++s) {
      qf[s] = *(const bf16x8*)(qp_ + 16 * ((2 * (s & 1) + hi) ^ ((r32 >> 2) & 3)) + 512 * (s >> 1));
    }
  }
  ldk(0);
  f32x16 scur = qk();
  float mScur = mref;
  SB();
  issue(1);
#pragma unroll
  for (int t = 0; t < NT - 1; ++t) {
    if (t == 0) wait_bar<4>(); else if (t < NT - 3) wait_bar<8>(); else if (t == NT - 3) wait_bar<6>(); else wait_bar<2>();
    ldk(t + 1);
    SB();
    const float mx = rowmax(scur);
    if (t > 0) {
      pv();
#pragma unroll
      for (int i = 0; i < 8; ++i) {
        __builtin_amdgcn_sched_group_barrier(0x8, 1, 1);
        __builtin_amdgcn_sched_group_barrier(0x402, 4, 1);
      }
    }
    SB();
    ldv(t);
    SB();
    fixmax(scur, mx, mScur);
    SB();
    const float mSn = mref;
    f32x16 snext = qk();
    expo(scur);
#pragma unroll
    for (int i = 0; i < 8; ++i) {
      __builtin_amdgcn_sched_group_barrier(0x8, 1, 0);
      __builtin_amdgcn_sched_group_barrier(0x402, 5, 0);
    }
    SB();
    scur = snext; mScur = mSn;
    if (t == 0) issue(2);
    if (t + 3 < NT) issue(t + 3);
  }
  wait_bar<0>();
  bf16x8 vg[4][2];
  {
    const char* sp = smem + ((NT - 1) % NSLOT) * SLOT;
#pragma unroll
    for (int s = 0; s < 2; ++s)
#pragma unroll
      for (int db = 0; db < 4; ++db) {
        s16x4 v0 = __builtin_amdgcn_ds_read_tr16_b64_v4i16((LDS_AS s16x4*)(sp + va0 + 4096 * s + 512 * db));
        s16x4 v1 = __builtin_amdgcn_ds_read_tr16_b64_v4i16((LDS_AS s16x4*)(sp + va1 + 4096 * s + 512 * db));
        vg[db][s] = __builtin_shufflevector(v0, v1, 0, 1, 2, 3, 4, 5, 6, 7);
      }
  }
  bf16x8 pg[2];
  auto expo2 = [&](f32x16& sc) {
    float ps = 0.f;
#pragma unroll
    for (int r = 0; r < 16; ++r) { sc[r] = __builtin_amdgcn_exp2f(sc[r]); ps += sc[r]; }
    l += ps;
#pragma unroll
    for (int s = 0; s < 2; ++s) {
      u32x4 w = {pk2(sc[8 * s + 0], sc[8 * s + 1]), pk2(sc[8 * s + 2], sc[8 * s + 3]), pk2(sc[8 * s + 4], sc[8 * s + 5]), pk2(sc[8 * s + 6], sc[8 * s + 7])};
      pg[s] = __builtin_bit_cast(bf16x8, w);
    }
  };
  {
    const float mx = rowmax(scur);
    const bool slow = !(mx <= THR) || (mScur != m);
    if (__builtin_expect(__builtin_amdgcn_ballot_w64(slow) != 0, 0)) {
      pv();
      SB();
      fixmax(scur, mx, mScur);
      expo2(scur);
    } else {
      pv();
      expo2(scur);
#pragma unroll
      for (int i = 0; i < 8; ++i) {
        __builtin_amdgcn_sched_group_barrier(0x8, 1, 2);
        __builtin_amdgcn_sched_group_barrier(0x402, 5, 2);
      }
    }
  }
  SB();
  const int pw = wid ^ 4;
  char* xb = smem + wid * 4096;
  float* stats = (float*)(smem + LDS_Q);
  if (kh) {
#pragma unroll
    for (int s = 0; s < 2; ++s) { o[0] = __builtin_amdgcn_mfma_f32_32x32x16_bf16(vg[0][s], pg[s], o[0], 0, 0, 0); o[1] = __builtin_amdgcn_mfma_f32_32x32x16_bf16(vg[1][s], pg[s], o[1], 0, 0, 0); }
#pragma unroll
    for (int s = 0; s < 2; ++s) { o[2] = __builtin_amdgcn_mfma_f32_32x32x16_bf16(vg[2][s], pg[s], o[2], 0, 0, 0); o[3] = __builtin_amdgcn_mfma_f32_32x32x16_bf16(vg[3][s], pg[s], o[3], 0, 0, 0); }
  } else {
#pragma unroll
    for (int s = 0; s < 2; ++s) { o[2] = __builtin_amdgcn_mfma_f32_32x32x16_bf16(vg[2][s], pg[s], o[2], 0, 0, 0); o[3] = __builtin_amdgcn_mfma_f32_32x32x16_bf16(vg[3][s], pg[s], o[3], 0, 0, 0); }
#pragma unroll
    for (int s = 0; s < 2; ++s) { o[0] = __builtin_amdgcn_mfma_f32_32x32x16_bf16(vg[0][s], pg[s], o[0], 0, 0, 0); o[1] = __builtin_amdgcn_mfma_f32_32x32x16_bf16(vg[1][s], pg[s], o[1], 0, 0, 0); }
  }
  {
    auto rr = __builtin_amdgcn_permlane32_swap(__float_as_uint(l), __float_as_uint(l), false, false);
    l = __uint_as_float(rr[0]) + __uint_as_float(rr[1]);
  }
  m *= CEXP;
  if (hi == 0) { stats[wid * 64 + r32] = m; stats[wid * 64 + 32 + r32] = l; }
#pragma unroll
  for (int j = 0; j < 2; ++j) {
    const f32x16& src = kh ? o[j] : o[2 + j];
#pragma unroll
    for (int pr = 0; pr < 2; ++pr) {
      u32x4 w = {pk2(src[8 * pr + 0], src[8 * pr + 1]), pk2(src[8 * pr + 2], src[8 * pr + 3]), pk2(src[8 * pr + 4], src[8 * pr + 5]), pk2(src[8 * pr + 6], src[8 * pr + 7])};
      *(u32x4*)(xb + (j * 2 + pr) * 1024 + lane * 16) = w;
    }
  }
  __syncthreads();
  const float m2 = stats[pw * 64 + r32], l2 = stats[pw * 64 + 32 + r32];
  const char* pb = smem + pw * 4096;
  u32x4 rc[2][2];
#pragma unroll
  for (int j = 0; j < 2; ++j)
#pragma unroll
    for (int pr = 0; pr < 2; ++pr) rc[j][pr] = *(const u32x4*)(pb + (j * 2 + pr) * 1024 + lane * 16);
  const float M = fmaxf(m, m2);
  const float e1 = __builtin_amdgcn_exp2f(m - M), e2 = __builtin_amdgcn_exp2f(m2 - M);
  const float L = l * e1 + l2 * e2;
  const float rL = 1.f / L;
  const float f1 = e1 * rL, f2 = e2 * rL;
  __amdgpu_buffer_rsrc_t rW = __builtin_amdgcn_make_buffer_rsrc((void*)ws, 0, WS_END, 0x00020000);
  const bool own_head = (g == split);
#pragma unroll
  for (int j = 0; j < 2; ++j) {
    const f32x16& own = kh ? o[2 + j] : o[j];
#pragma unroll
    for (int pr = 0; pr < 2; ++pr) {
      const u32x4 a = rc[j][pr];
      u32x4 w = {pk2(own[8 * pr + 0] * f1 + bflo(a[0]) * f2, own[8 * pr + 1] * f1 + bfhi(a[0]) * f2), pk2(own[8 * pr + 2] * f1 + bflo(a[1]) * f2, own[8 * pr + 3] * f1 + bfhi(a[1]) * f2),
                 pk2(own[8 * pr + 4] * f1 + bflo(a[2]) * f2, own[8 * pr + 5] * f1 + bfhi(a[2]) * f2), pk2(own[8 * pr + 6] * f1 + bflo(a[3]) * f2, own[8 * pr + 7] * f1 + bfhi(a[3]) * f2)};
      const int db = 2 * kh + j;
      if (own_head) *(u32x4*)(smem + LDS_OWN + (db * 2 + pr) * 1024 + lane * 16) = w;
      else __builtin_amdgcn_raw_buffer_store_b128(w, rW, WS_PO + (unsigned)((bid * 4 + g) * 8192 + (db * 2 + pr) * 1024 + lane * 16), 0, 16);
    }
  }
  {
    const unsigned long long tag = (gqa_dispatch_id() + 1ull) * 0x9E3779B97F4A7C15ull ^ 0x5DEECE66DA3C1F27ull;
    if (own_head) {
      if (kh == 0) {
        if (hi == 0) { float* st = (float*)(smem + LDS_OWN + 8192); st[r32] = M; st[32 + r32] = L; }
        if (lane < 6) {
          const int src_ = ((split + 1 + (lane >> 1)) & 3) * 2 + (lane & 1);
          unsigned long long* fin = (unsigned long long*)(ws + WS_FL + ((grp * 4 + split) * 8 + src_) * 64);
          unsigned spins = 0;
          while (__hip_atomic_load(fin, __ATOMIC_RELAXED, __HIP_MEMORY_SCOPE_AGENT) != tag) {
            __builtin_amdgcn_s_sleep(1);
            if (++spins > (1u << 18)) break;
          }
          __hip_atomic_store(fin, 0ull, __ATOMIC_RELAXED, __HIP_MEMORY_SCOPE_AGENT);
        }
      }
    } else {
      if (kh == 0 && hi == 0) {
        __builtin_amdgcn_raw_buffer_store_b32(__float_as_uint(M), rW, WS_ST + (unsigned)((bid * 4 + g) * 256 + r32 * 4), 0, 16);
        __builtin_amdgcn_raw_buffer_store_b32(__float_as_uint(L), rW, WS_ST + (unsigned)((bid * 4 + g) * 256 + 128 + r32 * 4), 0, 16);
      }
      asm volatile("s_waitcnt vmcnt(0)" ::: "memory");
      if (lane == 0) __hip_atomic_store((unsigned long long*)(ws + WS_FL + (((grp * 4 + g) * 4 + split) * 2 + kh) * 64), tag, __ATOMIC_RELAXED, __HIP_MEMORY_SCOPE_AGENT);
    }
  }
  __syncthreads();
  {
    u32x4 p[4]; float ms[4], ls[4];
#pragma unroll
    for (int s = 0; s < 4; ++s) {
      const int sp_ = (split + s) & 3;
      if (s == 0) {
        p[0] = *(const u32x4*)(smem + LDS_OWN + wid * 1024 + lane * 16);
        const float* st = (const float*)(smem + LDS_OWN + 8192);
        ms[0] = st[r32]; ls[0] = st[32 + r32];
      } else {
        const unsigned u = (unsigned)((grp * 4 + sp_) * 4 + split);
        p[s] = __builtin_amdgcn_raw_buffer_load_b128(rW, WS_PO + u * 8192 + wid * 1024 + lane * 16, 0, 16);
        ms[s] = __uint_as_float(__builtin_amdgcn_raw_buffer_load_b32(rW, WS_ST + u * 256 + r32 * 4, 0, 16));
        ls[s] = __uint_as_float(__builtin_amdgcn_raw_buffer_load_b32(rW, WS_ST + u * 256 + 128 + r32 * 4, 0, 16));
      }
    }
    const float Mx = fmaxf(fmaxf(ms[0], ms[1]), fmaxf(ms[2], ms[3]));
    float wt[4], W = 0.f;
#pragma unroll
    for (int s = 0; s < 4; ++s) { wt[s] = ls[s] * __builtin_amdgcn_exp2f(ms[s] - Mx); W += wt[s]; }
    const float rWt = 1.f / W;
    float acc[8];
#pragma unroll
    for (int j = 0; j < 8; ++j) acc[j] = 0.f;
#pragma unroll
    for (int s = 0; s < 4; ++s) {
      const float ww = wt[s] * rWt;
#pragma unroll
      for (int j = 0; j < 4; ++j) { acc[2 * j] += ww * bflo(p[s][j]); acc[2 * j + 1] += ww * bfhi(p[s][j]); }
    }
    const int db = wid >> 1, pr = wid & 1;
    __hip_bfloat16* orow = Op + ((size_t)(grp * 4 + split) * SQ + r32) * D + db * 32 + pr * 16 + 4 * hi;
    u32x2 a = {pk2(acc[0], acc[1]), pk2(acc[2], acc[3])};
    u32x2 b = {pk2(acc[4], acc[5]), pk2(acc[6], acc[7])};
    *(u32x2*)(orow) = a;
    *(u32x2*)(orow + 8) = b;
  }
}

__global__ __launch_bounds__(64) void gqa_code_end() {}

extern "C" void kernel_launch(void* const* d_in, const int* in_sizes, int n_in, void* d_out, int out_size, void* d_ws, size_t ws_size,
                              hipStream_t stream) {
  const __hip_bfloat16* q = (const __hip_bfloat16*)d_in[0];
  const __hip_bfloat16* k = (const __hip_bfloat16*)d_in[1];
  const __hip_bfloat16* v = (const __hip_bfloat16*)d_in[2];
  __hip_bfloat16* o = (__hip_bfloat16*)d_out;
  hipLaunchKernelGGL(gqa_split_kernel, dim3(256), dim3(512), 0, stream, q, k, v, (char*)d_ws, o);
}
```

```cpp
#include <hip/hip_runtime.h>
#include <hip/hip_bf16.h>
#include <stdint.h>

typedef __attribute__((ext_vector_type(8))) short bf16x8;
typedef __attribute__((ext_vector_type(4))) short s16x4;
typedef __attribute__((ext_vector_type(16))) float f32x16;
typedef __attribute__((ext_vector_type(4))) float f32x4;
typedef __attribute__((ext_vector_type(2))) float f32x2;
typedef __attribute__((ext_vector_type(2))) __bf16 bf16x2_t;
typedef __attribute__((ext_vector_type(4))) unsigned u32x4;
typedef __attribute__((ext_vector_type(2))) unsigned u32x2;

constexpr int Bn = 8, HQ = 32, HKV = 8, SQ = 32, SK = 2048, D = 128;
constexpr int NSPLIT = 4, KPS = SK / NSPLIT;
constexpr int TK = 64;
constexpr int NT = KPS / TK;
constexpr int NSLOT = 4;
constexpr int HALF = TK * D * 2;
constexpr int SLOT = 2 * HALF;
constexpr int LDS_Q = NSLOT * SLOT;
constexpr int LDS_BYTES = NSLOT * SLOT + 32768;
constexpr float CEXP = 0.088388347648318447f * 1.4426950408889634f;
constexpr float RCEXP = 1.f / CEXP;
constexpr float THR = 10.f;
constexpr unsigned WS_PO = 0;
constexpr unsigned WS_ST = 64 * 4 * 4 * 8192;
constexpr unsigned WS_FL = WS_ST + 64 * 4 * 4 * 256;
constexpr unsigned WS_END = WS_FL + 64 * 32 * 64;
constexpr int LDS_OWN = 65536;

extern "C" __device__ unsigned long long gqa_dispatch_id() __asm("llvm.amdgcn.dispatch.id");
#define LDS_AS __attribute__((address_space(3)))
#define SB() __builtin_amdgcn_sched_barrier(0)

__device__ __forceinline__ unsigned pk2(float a, float b) {
  f32x2 v = {a, b};
  bf16x2_t r = __builtin_convertvector(v, bf16x2_t);
  return __builtin_bit_cast(unsigned, r);
}
__device__ __forceinline__ float bflo(unsigned u) { return __uint_as_float(u << 16); }
__device__ __forceinline__ float bfhi(unsigned u) { return __uint_as_float(u & 0xffff0000u); }

__device__ __forceinline__ void dma2k(__amdgpu_buffer_rsrc_t rsrc, unsigned voff, unsigned soff, unsigned ldsaddr) {
  unsigned keep;
  asm volatile("s_mov_b32 %0, m0\n\ts_mov_b32 m0, %1\n\ts_nop 0\n\tbuffer_load_dwordx4 %2, %3, %4 offen nt lds\n\t"
               "s_add_u32 m0, m0, 0x380\n\ts_nop 0\n\tbuffer_load_dwordx4 %2, %3, %4 offen offset:128 nt lds\n\ts_mov_b32 m0, %0"
               : "=&s"(keep) : "s"(ldsaddr), "v"(voff), "s"(rsrc), "s"(soff) : "memory", "scc");
}

__device__ __forceinline__ void dma2k_def(__amdgpu_buffer_rsrc_t rsrc, unsigned voff, unsigned soff, unsigned ldsaddr) {
  unsigned keep;
  asm volatile("s_mov_b32 %0, m0\n\ts_mov_b32 m0, %1\n\ts_nop 0\n\tbuffer_load_dwordx4 %2, %3, %4 offen lds\n\t"
               "s_add_u32 m0, m0, 0x380\n\ts_nop 0\n\tbuffer_load_dwordx4 %2, %3, %4 offen offset:128 lds\n\ts_mov_b32 m0, %0"
               : "=&s"(keep) : "s"(ldsaddr), "v"(voff), "s"(rsrc), "s"(soff) : "memory", "scc");
}

template <int N> __device__ __forceinline__ void wait_bar() {
  if constexpr (N == 8) asm volatile("s_waitcnt vmcnt(16) lgkmcnt(0)\n\ts_barrier" ::: "memory");
  else if constexpr (N == 6) asm volatile("s_waitcnt vmcnt(12) lgkmcnt(0)\n\ts_barrier" ::: "memory");
  else if constexpr (N == 2) asm volatile("s_waitcnt vmcnt(4) lgkmcnt(0)\n\ts_barrier" ::: "memory");
  else if constexpr (N == 4) asm volatile("s_waitcnt vmcnt(8) lgkmcnt(0)\n\ts_barrier" ::: "memory");
  else asm volatile("s_waitcnt vmcnt(0) lgkmcnt(0)\n\ts_barrier" ::: "memory");
}

__global__ __launch_bounds__(512, 2) void gqa_split_kernel(const __hip_bfloat16* __restrict__ Qp, const __hip_bfloat16* __restrict__ Kp,
                                                           const __hip_bfloat16* __restrict__ Vp, char* __restrict__ ws, __hip_bfloat16* __restrict__ Op) {
  __shared__ __attribute__((aligned(16))) char smem[LDS_BYTES];
  const int tid = threadIdx.x, lane = tid & 63;
  const int wid = __builtin_amdgcn_readfirstlane(tid >> 6);
  const int g = wid & 3, kh = wid >> 2;
  const int r32 = lane & 31, hi = lane >> 5;
  const int xcd = blockIdx.x & 7, idx = blockIdx.x >> 3;
  const int grp = xcd * 8 + (idx >> 2), split = idx & 3;
  const int bid = grp * 4 + split;
  const size_t kvoff = ((size_t)grp * SK + (size_t)split * KPS) * D;
  __amdgpu_buffer_rsrc_t rK = __builtin_amdgcn_make_buffer_rsrc((void*)(Kp + kvoff), 0, KPS * D * 2, 0x00020000);
  __amdgpu_buffer_rsrc_t rV = __builtin_amdgcn_make_buffer_rsrc((void*)(Vp + kvoff), 0, KPS * D * 2, 0x00020000);
  const unsigned ldsbase = (unsigned)(uintptr_t)(LDS_AS char*)smem;

  const int dsub = lane >> 5, drr = (lane >> 2) & 7, dx = lane & 3;
  const unsigned dsrcA = (16 * g + drr) * 256 + (4 * dsub + (dx ^ ((drr >> 2) & 3))) * 16;
  const unsigned dsrcB = (16 * g + 8 + drr) * 256 + (4 * dsub + (dx ^ ((2 + (drr >> 2)) & 3))) * 16;
  auto issue = [&](int j) {
    if (kh == 0) {
      const unsigned lb = ldsbase + (j % NSLOT) * SLOT + g * 4096;
      if (j + 1 < NT) { dma2k(rK, dsrcA, (j + 1) * HALF, lb); dma2k(rK, dsrcB, (j + 1) * HALF, lb + 2048); }
      dma2k(rV, dsrcA, j * HALF, lb + HALF); dma2k(rV, dsrcB, j * HALF, lb + HALF + 2048);
    }
  };
  __amdgpu_buffer_rsrc_t rQ = __builtin_amdgcn_make_buffer_rsrc((void*)(Qp + (size_t)grp * 4 * SQ * D), 0, 4 * SQ * D * 2, 0x00020000);

  if (kh == 0) {
    const unsigned lb = ldsbase + (NSLOT - 1) * SLOT + g * 4096;
    dma2k(rK, dsrcA, 0, lb); dma2k(rK, dsrcB, 0, lb + 2048);
  } else {
    const unsigned lb = ldsbase + LDS_Q + g * 4096;
    dma2k_def(rQ, dsrcA, 0, lb); dma2k_def(rQ, dsrcB, 0, lb + 2048);
    dma2k_def(rQ, dsrcA, HALF, lb + HALF); dma2k_def(rQ, dsrcB, HALF, lb + HALF + 2048);
  }
  u32x4 pfd0, pfd1, pfd2, pfd3; unsigned pft = 0;
  if (kh) {
    unsigned pclo, pchi;
    asm volatile("s_getpc_b64 s[40:41]\n\ts_add_u32 s40, s40, _Z16gqa_split_kernelPK14__hip_bfloat16S1_S1_PcPS_@rel32@lo+4\n\ts_addc_u32 s41, s41, _Z16gqa_split_kernelPK14__hip_bfloat16S1_S1_PcPS_@rel32@hi+12\n\ts_mov_b32 %0, s40\n\ts_mov_b32 %1, s41"
                 : "=s"(pclo), "=s"(pchi) :: "scc", "s40", "s41");
    const unsigned long long pc = ((unsigned long long)pchi << 32) | pclo;
    unsigned pelo, pehi;
    asm volatile("s_getpc_b64 s[40:41]\n\ts_add_u32 s40, s40, _Z12gqa_code_endv@rel32@lo+4\n\ts_addc_u32 s41, s41, _Z12gqa_code_endv@rel32@hi+12\n\ts_mov_b32 %0, s40\n\ts_mov_b32 %1, s41"
                 : "=s"(pelo), "=s"(pehi) :: "scc", "s40", "s41");
    const unsigned long long pe = (((unsigned long long)pehi << 32) | pelo) - 16;
    const unsigned long long a0 = pc + g * 4096 + lane * 16;
    const char* c0 = (const char*)(a0 < pe ? a0 : pe);
    const char* c1 = (const char*)(a0 + 1024 < pe ? a0 + 1024 : pe);
    const char* c2 = (const char*)(a0 + 2048 < pe ? a0 + 2048 : pe);
    const char* c3 = (const char*)(a0 + 3072 < pe ? a0 + 3072 : pe);
    asm volatile("global_load_dwordx4 %0, %4, off\n\tglobal_load_dwordx4 %1, %5, off\n\tglobal_load_dwordx4 %2, %6, off\n\tglobal_load_dwordx4 %3, %7, off"
                 : "=&v"(pfd0), "=&v"(pfd1), "=&v"(pfd2), "=&v"(pfd3) : "v"(c0), "v"(c1), "v"(c2), "v"(c3) : "memory");
    {
      const char* pt = (g == 0) ? (const char*)(ws + WS_PO + (size_t)bid * 32768) : (g == 1) ? (const char*)(ws + WS_ST + (size_t)bid * 1024)
                     : (g == 2) ? (const char*)(ws + WS_FL + (size_t)bid * 512) : (const char*)(Op + (size_t)bid * SQ * D);
      asm volatile("global_load_dword %0, %1, off" : "=&v"(pft) : "v"(pt + (lane & 7) * 64) : "memory");
    }
  }
  __builtin_amdgcn_s_barrier();
  issue(0);

  const int krow = 32 * kh + r32;
  const int ka0 = 2048 * (krow >> 3) + 64 * (r32 & 7) + 16 * ((0 + hi) ^ ((r32 >> 2) & 3));
  const int ka1 = 2048 * (krow >> 3) + 64 * (r32 & 7) + 16 * ((2 + hi) ^ ((r32 >> 2) & 3));
  const int gq = lane >> 4, i16 = lane & 15, qp = i16 >> 2, pp = i16 & 3;
  const int vh = gq >> 1, vc = 2 * (gq & 1) + (pp >> 1);
  const int va0 = HALF + 8192 * kh + 64 * (4 * vh + qp) + 16 * (vc ^ vh) + 8 * (pp & 1);
  const int va1 = HALF + 8192 * kh + 2048 + 64 * (4 * vh + qp) + 16 * (vc ^ (2 + vh)) + 8 * (pp & 1);
  const int va0k = va0 + 1024 * kh, va1k = va1 + 1024 * kh, va0s = va0 + 1024 * (1 - kh), va1s = va1 + 1024 * (1 - kh);

  f32x16 o[4];
#pragma unroll
  for (int i = 0; i < 4; ++i)
#pragma unroll
    for (int r = 0; r < 16; ++r) o[i][r] = 0.f;
  float m = -__builtin_inff(), l = 0.f;
  f32x16 negm;
#pragma unroll
  for (int r = 0; r < 16; ++r) negm[r] = 0.f;
  float mref = 0.f;

  bf16x8 qf[8];
  bf16x8 kf[8];
  bf16x8 vf[4][2];
  auto ldk = [&](int t) {
    const char* sp = smem + ((t + NSLOT - 1) % NSLOT) * SLOT;
#pragma unroll
    for (int s = 0; s < 8; ++s) kf[s] = *(const bf16x8*)(sp + ((s & 1) ? ka1 : ka0) + 512 * (s >> 1));
  };
  auto ldv = [&](int t) {
    const char* sp = smem + (t % NSLOT) * SLOT;
#pragma unroll
    for (int s = 0; s < 2; ++s)
#pragma unroll
      for (int db = 0; db < 4; ++db) {
        s16x4 v0 = __builtin_amdgcn_ds_read_tr16_b64_v4i16((LDS_AS s16x4*)(sp + (db < 2 ? va0k : va0s) + 4096 * s + 512 * (db & 1)));
        s16x4 v1 = __builtin_amdgcn_ds_read_tr16_b64_v4i16((LDS_AS s16x4*)(sp + (db < 2 ? va1k : va1s) + 4096 * s + 512 * (db & 1)));
        vf[db][s] = __builtin_shufflevector(v0, v1, 0, 1, 2, 3, 4, 5, 6, 7);
      }
  };
  auto qk = [&]() {
    f32x16 sc = __builtin_amdgcn_mfma_f32_32x32x16_bf16(kf[0], qf[0], negm, 0, 0, 0);
#pragma unroll
    for (int s = 1; s < 8; ++s) sc = __builtin_amdgcn_mfma_f32_32x32x16_bf16(kf[s], qf[s], sc, 0, 0, 0);
    return sc;
  };
  auto rowmax = [&](f32x16& sc) {
#pragma unroll
    for (int r = 0; r < 16; ++r) sc[r] *= CEXP;
    float mx = sc[0];
#pragma unroll
    for (int r = 1; r < 16; ++r) mx = fmaxf(mx, sc[r]);
    auto rr = __builtin_amdgcn_permlane32_swap(__float_as_uint(mx), __float_as_uint(mx), false, false);
    return fmaxf(__uint_as_float(rr[0]), __uint_as_float(rr[1]));
  };
  auto fixmax = [&](f32x16& sc, float mx, float mS) {
    const bool slow = !(mx <= THR) || (mS != m);
    if (__builtin_expect(__builtin_amdgcn_ballot_w64(slow) != 0, 0)) {
      const float mn = fmaxf(m, mS + mx * RCEXP);
      const float alpha = __builtin_amdgcn_exp2f((m - mn) * CEXP);
      const float dlt = (mS - mn) * CEXP;
      m = mn; mref = mn;
      l *= alpha;
#pragma unroll
      for (int r = 0; r < 16; ++r) { sc[r] += dlt; negm[r] = -mn; }
#pragma unroll
      for (int db = 0; db < 4; ++db)
#pragma unroll
        for (int r = 0; r < 16; ++r) o[db][r] *= alpha;
    }
  };
  bf16x8 pf[2];
  auto expo = [&](f32x16& sc) {
    float ps = 0.f;
#pragma unroll
    for (int r = 0; r < 16; ++r) { sc[r] = __builtin_amdgcn_exp2f(sc[r]); ps += sc[r]; }
    l += ps;
#pragma unroll
    for (int s = 0; s < 2; ++s) {
      u32x4 w = {pk2(sc[8 * s + 0], sc[8 * s + 1]), pk2(sc[8 * s + 2], sc[8 * s + 3]), pk2(sc[8 * s + 4], sc[8 * s + 5]), pk2(sc[8 * s + 6], sc[8 * s + 7])};
      pf[s] = __builtin_bit_cast(bf16x8, w);
    }
  };
  auto pv = [&]() {
#pragma unroll
    for (int s = 0; s < 2; ++s)
#pragma unroll
      for (int db = 0; db < 4; ++db) o[db] = __builtin_amdgcn_mfma_f32_32x32x16_bf16(vf[db][s], pf[s], o[db], 0, 0, 0);
  };
  if (kh) asm volatile("s_waitcnt vmcnt(0)" : "+v"(pfd0), "+v"(pfd1), "+v"(pfd2), "+v"(pfd3), "+v"(pft) :: "memory");
  wait_bar<4>();
  {
    const int qrow = 32 * g + r32;
    const char* qp_ = smem + LDS_Q + 2048 * (qrow >> 3) + 64 * (r32 & 7);
#pragma unroll
    for (int s = 0; s < 8; ++s) {
      qf[s] = *(const bf16x8*)(qp_ + 16 * ((2 * (s & 1) + hi) ^ ((r32 >> 2) & 3)) + 512 * (s >> 1));
    }
  }
  ldk(0);
  f32x16 scur = qk();
  float mScur = mref;
  SB();
  issue(1);
#pragma unroll
  for (int t = 0; t < NT - 1; ++t) {
    if (t == 0) wait_bar<4>(); else if (t < NT - 3) wait_bar<8>(); else if (t == NT - 3) wait_bar<6>(); else wait_bar<2>();
    ldk(t + 1);
    SB();
    const float mx = rowmax(scur);
    if (t > 0) {
      pv();
#pragma unroll
      for (int i = 0; i < 8; ++i) {
        __builtin_amdgcn_sched_group_barrier(0x8, 1, 1);
        __builtin_amdgcn_sched_group_barrier(0x402, 4, 1);
      }
    }
    SB();
    ldv(t);
    SB();
    fixmax(scur, mx, mScur);
    SB();
    const float mSn = mref;
    f32x16 snext = qk();
    expo(scur);
#pragma unroll
    for (int i = 0; i < 8; ++i) {
      __builtin_amdgcn_sched_group_barrier(0x8, 1, 0);
      __builtin_amdgcn_sched_group_barrier(0x402, 5, 0);
    }
    SB();
    scur = snext; mScur = mSn;
    if (t == 0) issue(2);
    if (t + 3 < NT) issue(t + 3);
  }
  wait_bar<0>();
  bf16x8 vg[4][2];
  {
    const char* sp = smem + ((NT - 1) % NSLOT) * SLOT;
#pragma unroll
    for (int s = 0; s < 2; ++s)
#pragma unroll
      for (int db = 0; db < 4; ++db) {
        s16x4 v0 = __builtin_amdgcn_ds_read_tr16_b64_v4i16((LDS_AS s16x4*)(sp + (db < 2 ? va0k : va0s) + 4096 * s + 512 * (db & 1)));
        s16x4 v1 = __builtin_amdgcn_ds_read_tr16_b64_v4i16((LDS_AS s16x4*)(sp + (db < 2 ? va1k : va1s) + 4096 * s + 512 * (db & 1)));
        vg[db][s] = __builtin_shufflevector(v0, v1, 0, 1, 2, 3, 4, 5, 6, 7);
      }
  }
  bf16x8 pg[2];
  auto expo2 = [&](f32x16& sc) {
    float ps = 0.f;
#pragma unroll
    for (int r = 0; r < 16; ++r) { sc[r] = __builtin_amdgcn_exp2f(sc[r]); ps += sc[r]; }
    l += ps;
#pragma unroll
    for (int s = 0; s < 2; ++s) {
      u32x4 w = {pk2(sc[8 * s + 0], sc[8 * s + 1]), pk2(sc[8 * s + 2], sc[8 * s + 3]), pk2(sc[8 * s + 4], sc[8 * s + 5]), pk2(sc[8 * s + 6], sc[8 * s + 7])};
      pg[s] = __builtin_bit_cast(bf16x8, w);
    }
  };
  {
    const float mx = rowmax(scur);
    const float mold = m;
    fixmax(scur, mx, mScur);
    if (__builtin_expect(__builtin_amdgcn_ballot_w64(m != mold) != 0, 0)) {
      const float alpha = __builtin_amdgcn_exp2f((mold - m) * CEXP);
#pragma unroll
      for (int s = 0; s < 2; ++s) {
        u32x4 w = __builtin_bit_cast(u32x4, pf[s]);
#pragma unroll
        for (int j = 0; j < 4; ++j) w[j] = pk2(bflo(w[j]) * alpha, bfhi(w[j]) * alpha);
        pf[s] = __builtin_bit_cast(bf16x8, w);
      }
    }
    SB();
    pv();
    expo2(scur);
#pragma unroll
    for (int i = 0; i < 8; ++i) {
      __builtin_amdgcn_sched_group_barrier(0x8, 1, 2);
      __builtin_amdgcn_sched_group_barrier(0x402, 5, 2);
    }
  }
  SB();
  const int pw = wid ^ 4;
  char* xb = smem + wid * 4096;
  float* stats = (float*)(smem + LDS_Q);
#pragma unroll
  for (int s = 0; s < 2; ++s) { o[2] = __builtin_amdgcn_mfma_f32_32x32x16_bf16(vg[2][s], pg[s], o[2], 0, 0, 0); o[3] = __builtin_amdgcn_mfma_f32_32x32x16_bf16(vg[3][s], pg[s], o[3], 0, 0, 0); }
#pragma unroll
  for (int s = 0; s < 2; ++s) { o[0] = __builtin_amdgcn_mfma_f32_32x32x16_bf16(vg[0][s], pg[s], o[0], 0, 0, 0); o[1] = __builtin_amdgcn_mfma_f32_32x32x16_bf16(vg[1][s], pg[s], o[1], 0, 0, 0); }
  {
    auto rr = __builtin_amdgcn_permlane32_swap(__float_as_uint(l), __float_as_uint(l), false, false);
    l = __uint_as_float(rr[0]) + __uint_as_float(rr[1]);
  }
  m *= CEXP;
  if (hi == 0) { stats[wid * 64 + r32] = m; stats[wid * 64 + 32 + r32] = l; }
#pragma unroll
  for (int j = 0; j < 2; ++j) {
    const f32x16& src = o[2 + j];
#pragma unroll
    for (int pr = 0; pr < 2; ++pr) {
      u32x4 w = {pk2(src[8 * pr + 0], src[8 * pr + 1]), pk2(src[8 * pr + 2], src[8 * pr + 3]), pk2(src[8 * pr + 4], src[8 * pr + 5]), pk2(src[8 * pr + 6], src[8 * pr + 7])};
      *(u32x4*)(xb + (j * 2 + pr) * 1024 + lane * 16) = w;
    }
  }
  __syncthreads();
  const float m2 = stats[pw * 64 + r32], l2 = stats[pw * 64 + 32 + r32];
  const char* pb = smem + pw * 4096;
  u32x4 rc[2][2];
#pragma unroll
  for (int j = 0; j < 2; ++j)
#pragma unroll
    for (int pr = 0; pr < 2; ++pr) rc[j][pr] = *(const u32x4*)(pb + (j * 2 + pr) * 1024 + lane * 16);
  const float M = fmaxf(m, m2);
  const float e1 = __builtin_amdgcn_exp2f(m - M), e2 = __builtin_amdgcn_exp2f(m2 - M);
  const float L = l * e1 + l2 * e2;
  const float rL = __builtin_amdgcn_rcpf(L);
  const float f1 = e1 * rL, f2 = e2 * rL;
  __amdgpu_buffer_rsrc_t rW = __builtin_amdgcn_make_buffer_rsrc((void*)ws, 0, WS_END, 0x00020000);
  const bool own_head = (g == split);
#pragma unroll
  for (int j = 0; j < 2; ++j) {
    const f32x16& own = o[j];
#pragma unroll
    for (int pr = 0; pr < 2; ++pr) {
      const u32x4 a = rc[j][pr];
      u32x4 w = {pk2(own[8 * pr + 0] * f1 + bflo(a[0]) * f2, own[8 * pr + 1] * f1 + bfhi(a[0]) * f2), pk2(own[8 * pr + 2] * f1 + bflo(a[1]) * f2, own[8 * pr + 3] * f1 + bfhi(a[1]) * f2),
                 pk2(own[8 * pr + 4] * f1 + bflo(a[2]) * f2, own[8 * pr + 5] * f1 + bfhi(a[2]) * f2), pk2(own[8 * pr + 6] * f1 + bflo(a[3]) * f2, own[8 * pr + 7] * f1 + bfhi(a[3]) * f2)};
      const int db = 2 * kh + j;
      if (own_head) *(u32x4*)(smem + LDS_OWN + (db * 2 + pr) * 1024 + lane * 16) = w;
      else __builtin_amdgcn_raw_buffer_store_b128(w, rW, WS_PO + (unsigned)((bid * 4 + g) * 8192 + (db * 2 + pr) * 1024 + lane * 16), 0, 16);
    }
  }
  {
    const unsigned long long tag = (gqa_dispatch_id() + 1ull) * 0x9E3779B97F4A7C15ull ^ 0x5DEECE66DA3C1F27ull;
    if (own_head) {
      if (kh == 0) {
        if (hi == 0) { float* st = (float*)(smem + LDS_OWN + 8192); st[r32] = M; st[32 + r32] = L; }
        if (lane < 6) {
          const int src_ = ((split + 1 + (lane >> 1)) & 3) * 2 + (lane & 1);
          unsigned long long* fin = (unsigned long long*)(ws + WS_FL + ((grp * 4 + split) * 8 + src_) * 64);
          unsigned spins = 0;
          while (__hip_atomic_load(fin, __ATOMIC_RELAXED, __HIP_MEMORY_SCOPE_AGENT) != tag) {
            __builtin_amdgcn_s_sleep(1);
            if (++spins > (1u << 18)) break;
          }
          __hip_atomic_store(fin, 0ull, __ATOMIC_RELAXED, __HIP_MEMORY_SCOPE_AGENT);
        }
      }
    } else {
      if (kh == 0 && hi == 0) {
        __builtin_amdgcn_raw_buffer_store_b32(__float_as_uint(M), rW, WS_ST + (unsigned)((bid * 4 + g) * 256 + r32 * 4), 0, 16);
        __builtin_amdgcn_raw_buffer_store_b32(__float_as_uint(L), rW, WS_ST + (unsigned)((bid * 4 + g) * 256 + 128 + r32 * 4), 0, 16);
      }
      asm volatile("s_waitcnt vmcnt(0)" ::: "memory");
      if (lane == 0) __hip_atomic_store((unsigned long long*)(ws + WS_FL + (((grp * 4 + g) * 4 + split) * 2 + kh) * 64), tag, __ATOMIC_RELAXED, __HIP_MEMORY_SCOPE_AGENT);
    }
  }
  __syncthreads();
  {
    u32x4 p[4]; float ms[4], ls[4];
#pragma unroll
    for (int s = 0; s < 4; ++s) {
      const int sp_ = (split + s) & 3;
      if (s == 0) {
        p[0] = *(const u32x4*)(smem + LDS_OWN + wid * 1024 + lane * 16);
        const float* st = (const float*)(smem + LDS_OWN + 8192);
        ms[0] = st[r32]; ls[0] = st[32 + r32];
      } else {
        const unsigned u = (unsigned)((grp * 4 + sp_) * 4 + split);
        p[s] = __builtin_amdgcn_raw_buffer_load_b128(rW, WS_PO + u * 8192 + wid * 1024 + lane * 16, 0, 16);
        ms[s] = __uint_as_float(__builtin_amdgcn_raw_buffer_load_b32(rW, WS_ST + u * 256 + r32 * 4, 0, 16));
        ls[s] = __uint_as_float(__builtin_amdgcn_raw_buffer_load_b32(rW, WS_ST + u * 256 + 128 + r32 * 4, 0, 16));
      }
    }
    const float Mx = fmaxf(fmaxf(ms[0], ms[1]), fmaxf(ms[2], ms[3]));
    float wt[4], W = 0.f;
#pragma unroll
    for (int s = 0; s < 4; ++s) { wt[s] = ls[s] * __builtin_amdgcn_exp2f(ms[s] - Mx); W += wt[s]; }
    const float rWt = __builtin_amdgcn_rcpf(W);
    float acc[8];
#pragma unroll
    for (int j = 0; j < 8; ++j) acc[j] = 0.f;
#pragma unroll
    for (int s = 0; s < 4; ++s) {
      const float ww = wt[s] * rWt;
#pragma unroll
      for (int j = 0; j < 4; ++j) { acc[2 * j] += ww * bflo(p[s][j]); acc[2 * j + 1] += ww * bfhi(p[s][j]); }
    }
    const int db = wid >> 1, pr = wid & 1;
    __hip_bfloat16* orow = Op + ((size_t)(grp * 4 + split) * SQ + r32) * D + db * 32 + pr * 16 + 4 * hi;
    u32x2 a = {pk2(acc[0], acc[1]), pk2(acc[2], acc[3])};
    u32x2 b = {pk2(acc[4], acc[5]), pk2(acc[6], acc[7])};
    *(u32x2*)(orow) = a;
    *(u32x2*)(orow + 8) = b;
  }
}

__global__ __launch_bounds__(64) void gqa_code_end() {}

extern "C" void kernel_launch(void* const* d_in, const int* in_sizes, int n_in, void* d_out, int out_size, void* d_ws, size_t ws_size,
                              hipStream_t stream) {
  const __hip_bfloat16* q = (const __hip_bfloat16*)d_in[0];
  const __hip_bfloat16* k = (const __hip_bfloat16*)d_in[1];
  const __hip_bfloat16* v = (const __hip_bfloat16*)d_in[2];
  __hip_bfloat16* o = (__hip_bfloat16*)d_out;
  hipLaunchKernelGGL(gqa_split_kernel, dim3(256), dim3(512), 0, stream, q, k, v, (char*)d_ws, o);
}
```

```cpp
#include <hip/hip_runtime.h>
#include <hip/hip_bf16.h>
#include <stdint.h>

typedef __attribute__((ext_vector_type(8))) short bf16x8;
typedef __attribute__((ext_vector_type(4))) short s16x4;
typedef __attribute__((ext_vector_type(16))) float f32x16;
typedef __attribute__((ext_vector_type(4))) float f32x4;
typedef __attribute__((ext_vector_type(2))) float f32x2;
typedef __attribute__((ext_vector_type(2))) __bf16 bf16x2_t;
typedef __attribute__((ext_vector_type(4))) unsigned u32x4;
typedef __attribute__((ext_vector_type(2))) unsigned u32x2;

constexpr int Bn = 8, HQ = 32, HKV = 8, SQ = 32, SK = 2048, D = 128;
constexpr int NSPLIT = 4, KPS = SK / NSPLIT;
constexpr int TK = 64;
constexpr int NT = KPS / TK;
constexpr int NSLOT = 4;
constexpr int HALF = TK * D * 2;
constexpr int SLOT = 2 * HALF;
constexpr int LDS_Q = NSLOT * SLOT;
constexpr int LDS_BYTES = NSLOT * SLOT + 32768;
constexpr float CEXP = 0.088388347648318447f * 1.4426950408889634f;
constexpr float RCEXP = 1.f / CEXP;
constexpr float THR = 10.f;
constexpr unsigned WS_PO = 0;
constexpr unsigned WS_ST = 64 * 4 * 4 * 8192;
constexpr unsigned WS_FL = WS_ST + 64 * 4 * 4 * 256;
constexpr unsigned WS_END = WS_FL + 64 * 32 * 64;
constexpr int LDS_OWN = 65536;

extern "C" __device__ unsigned long long gqa_dispatch_id() __asm("llvm.amdgcn.dispatch.id");
#define LDS_AS __attribute__((address_space(3)))
#define SB() __builtin_amdgcn_sched_barrier(0)

__device__ __forceinline__ unsigned pk2(float a, float b) {
  f32x2 v = {a, b};
  bf16x2_t r = __builtin_convertvector(v, bf16x2_t);
  return __builtin_bit_cast(unsigned, r);
}
__device__ __forceinline__ float bflo(unsigned u) { return __uint_as_float(u << 16); }
__device__ __forceinline__ float bfhi(unsigned u) { return __uint_as_float(u & 0xffff0000u); }

__device__ __forceinline__ void dma2k(__amdgpu_buffer_rsrc_t rsrc, unsigned voff, unsigned soff, unsigned ldsaddr) {
  unsigned keep;
  asm volatile("s_mov_b32 %0, m0\n\ts_mov_b32 m0, %1\n\ts_nop 0\n\tbuffer_load_dwordx4 %2, %3, %4 offen nt lds\n\t"
               "s_add_u32 m0, m0, 0x380\n\ts_nop 0\n\tbuffer_load_dwordx4 %2, %3, %4 offen offset:128 nt lds\n\ts_mov_b32 m0, %0"
               : "=&s"(keep) : "s"(ldsaddr), "v"(voff), "s"(rsrc), "s"(soff) : "memory", "scc");
}

__device__ __forceinline__ void dma2k_def(__amdgpu_buffer_rsrc_t rsrc, unsigned voff, unsigned soff, unsigned ldsaddr) {
  unsigned keep;
  asm volatile("s_mov_b32 %0, m0\n\ts_mov_b32 m0, %1\n\ts_nop 0\n\tbuffer_load_dwordx4 %2, %3, %4 offen lds\n\t"
               "s_add_u32 m0, m0, 0x380\n\ts_nop 0\n\tbuffer_load_dwordx4 %2, %3, %4 offen offset:128 lds\n\ts_mov_b32 m0, %0"
               : "=&s"(keep) : "s"(ldsaddr), "v"(voff), "s"(rsrc), "s"(soff) : "memory", "scc");
}

template <int N> __device__ __forceinline__ void wait_bar() {
  if constexpr (N == 8) asm volatile("s_waitcnt vmcnt(16) lgkmcnt(0)\n\ts_barrier" ::: "memory");
  else if constexpr (N == 6) asm volatile("s_waitcnt vmcnt(12) lgkmcnt(0)\n\ts_barrier" ::: "memory");
  else if constexpr (N == 2) asm volatile("s_waitcnt vmcnt(4) lgkmcnt(0)\n\ts_barrier" ::: "memory");
  else if constexpr (N == 4) asm volatile("s_waitcnt vmcnt(8) lgkmcnt(0)\n\ts_barrier" ::: "memory");
  else asm volatile("s_waitcnt vmcnt(0) lgkmcnt(0)\n\ts_barrier" ::: "memory");
}

__global__ __launch_bounds__(512, 2) void gqa_split_kernel(const __hip_bfloat16* __restrict__ Qp, const __hip_bfloat16* __restrict__ Kp,
                                                           const __hip_bfloat16* __restrict__ Vp, char* __restrict__ ws, __hip_bfloat16* __restrict__ Op) {
  __shared__ __attribute__((aligned(16))) char smem[LDS_BYTES];
  const int tid = threadIdx.x, lane = tid & 63;
  const int wid = __builtin_amdgcn_readfirstlane(tid >> 6);
  const int g = wid & 3, kh = wid >> 2;
  const int r32 = lane & 31, hi = lane >> 5;
  const int xcd = blockIdx.x & 7, idx = blockIdx.x >> 3;
  const int grp = xcd * 8 + (idx >> 2), split = idx & 3;
  const int bid = grp * 4 + split;
  const size_t kvoff = ((size_t)grp * SK + (size_t)split * KPS) * D;
  __amdgpu_buffer_rsrc_t rK = __builtin_amdgcn_make_buffer_rsrc((void*)(Kp + kvoff), 0, KPS * D * 2, 0x00020000);
  __amdgpu_buffer_rsrc_t rV = __builtin_amdgcn_make_buffer_rsrc((void*)(Vp + kvoff), 0, KPS * D * 2, 0x00020000);
  const unsigned ldsbase = (unsigned)(uintptr_t)(LDS_AS char*)smem;

  const int dsub = lane >> 5, drr = (lane >> 2) & 7, dx = lane & 3;
  const unsigned dsrcA = (16 * g + drr) * 256 + (4 * dsub + (dx ^ ((drr >> 2) & 3))) * 16;
  const unsigned dsrcB = (16 * g + 8 + drr) * 256 + (4 * dsub + (dx ^ ((2 + (drr >> 2)) & 3))) * 16;
  auto issue = [&](int j) {
    if (kh == 0) {
      const unsigned lb = ldsbase + (j % NSLOT) * SLOT + g * 4096;
      if (j + 1 < NT) { dma2k(rK, dsrcA, (j + 1) * HALF, lb); dma2k(rK, dsrcB, (j + 1) * HALF, lb + 2048); }
      dma2k(rV, dsrcA, j * HALF, lb + HALF); dma2k(rV, dsrcB, j * HALF, lb + HALF + 2048);
    }
  };
  __amdgpu_buffer_rsrc_t rQ = __builtin_amdgcn_make_buffer_rsrc((void*)(Qp + (size_t)grp * 4 * SQ * D), 0, 4 * SQ * D * 2, 0x00020000);

  if (kh == 0) {
    const unsigned lb = ldsbase + (NSLOT - 1) * SLOT + g * 4096;
    dma2k(rK, dsrcA, 0, lb); dma2k(rK, dsrcB, 0, lb + 2048);
  } else {
    const unsigned lb = ldsbase + LDS_Q + g * 4096;
    dma2k_def(rQ, dsrcA, 0, lb); dma2k_def(rQ, dsrcB, 0, lb + 2048);
    dma2k_def(rQ, dsrcA, HALF, lb + HALF); dma2k_def(rQ, dsrcB, HALF, lb + HALF + 2048);
  }
  u32x4 pfd0, pfd1, pfd2, pfd3;
  if (kh) {
    unsigned pclo, pchi;
    asm volatile("s_getpc_b64 s[40:41]\n\ts_add_u32 s40, s40, _Z16gqa_split_kernelPK14__hip_bfloat16S1_S1_PcPS_@rel32@lo+4\n\ts_addc_u32 s41, s41, _Z16gqa_split_kernelPK14__hip_bfloat16S1_S1_PcPS_@rel32@hi+12\n\ts_mov_b32 %0, s40\n\ts_mov_b32 %1, s41"
                 : "=s"(pclo), "=s"(pchi) :: "scc", "s40", "s41");
    const unsigned long long pc = ((unsigned long long)pchi << 32) | pclo;
    unsigned pelo, pehi;
    asm volatile("s_getpc_b64 s[40:41]\n\ts_add_u32 s40, s40, _Z12gqa_code_endv@rel32@lo+4\n\ts_addc_u32 s41, s41, _Z12gqa_code_endv@rel32@hi+12\n\ts_mov_b32 %0, s40\n\ts_mov_b32 %1, s41"
                 : "=s"(pelo), "=s"(pehi) :: "scc", "s40", "s41");
    const unsigned long long pe = (((unsigned long long)pehi << 32) | pelo) - 16;
    const unsigned long long a0 = pc + g * 4096 + lane * 16;
    const char* c0 = (const char*)(a0 < pe ? a0 : pe);
    const char* c1 = (const char*)(a0 + 1024 < pe ? a0 + 1024 : pe);
    const char* c2 = (const char*)(a0 + 2048 < pe ? a0 + 2048 : pe);
    const char* c3 = (const char*)(a0 + 3072 < pe ? a0 + 3072 : pe);
    asm volatile("global_load_dwordx4 %0, %4, off\n\tglobal_load_dwordx4 %1, %5, off\n\tglobal_load_dwordx4 %2, %6, off\n\tglobal_load_dwordx4 %3, %7, off"
                 : "=&v"(pfd0), "=&v"(pfd1), "=&v"(pfd2), "=&v"(pfd3) : "v"(c0), "v"(c1), "v"(c2), "v"(c3) : "memory");
  }
  __builtin_amdgcn_s_barrier();
  issue(0);

  const int krow = 32 * kh + r32;
  const int ka0 = 2048 * (krow >> 3) + 64 * (r32 & 7) + 16 * ((0 + hi) ^ ((r32 >> 2) & 3));
  const int ka1 = 2048 * (krow >> 3) + 64 * (r32 & 7) + 16 * ((2 + hi) ^ ((r32 >> 2) & 3));
  const int gq = lane >> 4, i16 = lane & 15, qp = i16 >> 2, pp = i16 & 3;
  const int vh = gq >> 1, vc = 2 * (gq & 1) + (pp >> 1);
  const int va0 = HALF + 8192 * kh + 64 * (4 * vh + qp) + 16 * (vc ^ vh) + 8 * (pp & 1);
  const int va1 = HALF + 8192 * kh + 2048 + 64 * (4 * vh + qp) + 16 * (vc ^ (2 + vh)) + 8 * (pp & 1);
  const int va0k = va0 + 1024 * kh, va1k = va1 + 1024 * kh, va0s = va0 + 1024 * (1 - kh), va1s = va1 + 1024 * (1 - kh);

  f32x16 o[4];
#pragma unroll
  for (int i = 0; i < 4; ++i)
#pragma unroll
    for (int r = 0; r < 16; ++r) o[i][r] = 0.f;
  float m = -__builtin_inff(), l = 0.f;
  f32x16 negm;
#pragma unroll
  for (int r = 0; r < 16; ++r) negm[r] = 0.f;
  float mref = 0.f;

  bf16x8 qf[8];
  bf16x8 kf[8];
  bf16x8 vf[4][2];
  auto ldk = [&](int t) {
    const char* sp = smem + ((t + NSLOT - 1) % NSLOT) * SLOT;
#pragma unroll
    for (int s = 0; s < 8; ++s) kf[s] = *(const bf16x8*)(sp + ((s & 1) ? ka1 : ka0) + 512 * (s >> 1));
  };
  auto ldv = [&](int t) {
    const char* sp = smem + (t % NSLOT) * SLOT;
#pragma unroll
    for (int s = 0; s < 2; ++s)
#pragma unroll
      for (int db = 0; db < 4; ++db) {
        s16x4 v0 = __builtin_amdgcn_ds_read_tr16_b64_v4i16((LDS_AS s16x4*)(sp + (db < 2 ? va0k : va0s) + 4096 * s + 512 * (db & 1)));
        s16x4 v1 = __builtin_amdgcn_ds_read_tr16_b64_v4i16((LDS_AS s16x4*)(sp + (db < 2 ? va1k : va1s) + 4096 * s + 512 * (db & 1)));
        vf[db][s] = __builtin_shufflevector(v0, v1, 0, 1, 2, 3, 4, 5, 6, 7);
      }
  };
  auto qk = [&]() {
    f32x16 sc = __builtin_amdgcn_mfma_f32_32x32x16_bf16(kf[0], qf[0], negm, 0, 0, 0);
#pragma unroll
    for (int s = 1; s < 8; ++s) sc = __builtin_amdgcn_mfma_f32_32x32x16_bf16(kf[s], qf[s], sc, 0, 0, 0);
    return sc;
  };
  auto rowmax = [&](f32x16& sc) {
#pragma unroll
    for (int r = 0; r < 16; ++r) sc[r] *= CEXP;
    float mx = sc[0];
#pragma unroll
    for (int r = 1; r < 16; ++r) mx = fmaxf(mx, sc[r]);
    auto rr = __builtin_amdgcn_permlane32_swap(__float_as_uint(mx), __float_as_uint(mx), false, false);
    return fmaxf(__uint_as_float(rr[0]), __uint_as_float(rr[1]));
  };
  auto fixmax = [&](f32x16& sc, float mx, float mS) {
    const bool slow = !(mx <= THR) || (mS != m);
    if (__builtin_expect(__builtin_amdgcn_ballot_w64(slow) != 0, 0)) {
      const float mn = fmaxf(m, mS + mx * RCEXP);
      const float alpha = __builtin_amdgcn_exp2f((m - mn) * CEXP);
      const float dlt = (mS - mn) * CEXP;
      m = mn; mref = mn;
      l *= alpha;
#pragma unroll
      for (int r = 0; r < 16; ++r) { sc[r] += dlt; negm[r] = -mn; }
#pragma unroll
      for (int db = 0; db < 4; ++db)
#pragma unroll
        for (int r = 0; r < 16; ++r) o[db][r] *= alpha;
    }
  };
  bf16x8 pf[2];
  auto expo = [&](f32x16& sc) {
    float ps = 0.f;
#pragma unroll
    for (int r = 0; r < 16; ++r) { sc[r] = __builtin_amdgcn_exp2f(sc[r]); ps += sc[r]; }
    l += ps;
#pragma unroll
    for (int s = 0; s < 2; ++s) {
      u32x4 w = {pk2(sc[8 * s + 0], sc[8 * s + 1]), pk2(sc[8 * s + 2], sc[8 * s + 3]), pk2(sc[8 * s + 4], sc[8 * s + 5]), pk2(sc[8 * s + 6], sc[8 * s + 7])};
      pf[s] = __builtin_bit_cast(bf16x8, w);
    }
  };
  auto pv = [&]() {
#pragma unroll
    for (int s = 0; s < 2; ++s)
#pragma unroll
      for (int db = 0; db < 4; ++db) o[db] = __builtin_amdgcn_mfma_f32_32x32x16_bf16(vf[db][s], pf[s], o[db], 0, 0, 0);
  };
  if (kh) asm volatile("s_waitcnt vmcnt(0)" : "+v"(pfd0), "+v"(pfd1), "+v"(pfd2), "+v"(pfd3) :: "memory");
  wait_bar<4>();
  {
    const int qrow = 32 * g + r32;
    const char* qp_ = smem + LDS_Q + 2048 * (qrow >> 3) + 64 * (r32 & 7);
#pragma unroll
    for (int s = 0; s < 8; ++s) {
      qf[s] = *(const bf16x8*)(qp_ + 16 * ((2 * (s & 1) + hi) ^ ((r32 >> 2) & 3)) + 512 * (s >> 1));
    }
  }
  ldk(0);
  f32x16 scur = qk();
  float mScur = mref;
  SB();
  issue(1);
#pragma unroll
  for (int t = 0; t < NT - 1; ++t) {
    if (t == 0) wait_bar<4>(); else if (t < NT - 3) wait_bar<8>(); else if (t == NT - 3) wait_bar<6>(); else wait_bar<2>();
    ldk(t + 1);
    SB();
    const float mx = rowmax(scur);
    if (t > 0) {
      pv();
#pragma unroll
      for (int i = 0; i < 8; ++i) {
        __builtin_amdgcn_sched_group_barrier(0x8, 1, 1);
        __builtin_amdgcn_sched_group_barrier(0x402, 4, 1);
      }
    }
    SB();
    ldv(t);
    SB();
    fixmax(scur, mx, mScur);
    SB();
    const float mSn = mref;
    f32x16 snext = qk();
    expo(scur);
#pragma unroll
    for (int i = 0; i < 8; ++i) {
      __builtin_amdgcn_sched_group_barrier(0x8, 1, 0);
      __builtin_amdgcn_sched_group_barrier(0x402, 5, 0);
    }
    SB();
    scur = snext; mScur = mSn;
    if (t == 0) issue(2);
    if (t + 3 < NT) issue(t + 3);
  }
  wait_bar<0>();
  bf16x8 vg[4][2];
  {
    const char* sp = smem + ((NT - 1) % NSLOT) * SLOT;
#pragma unroll
    for (int s = 0; s < 2; ++s)
#pragma unroll
      for (int db = 0; db < 4; ++db) {
        s16x4 v0 = __builtin_amdgcn_ds_read_tr16_b64_v4i16((LDS_AS s16x4*)(sp + (db < 2 ? va0k : va0s) + 4096 * s + 512 * (db & 1)));
        s16x4 v1 = __builtin_amdgcn_ds_read_tr16_b64_v4i16((LDS_AS s16x4*)(sp + (db < 2 ? va1k : va1s) + 4096 * s + 512 * (db & 1)));
        vg[db][s] = __builtin_shufflevector(v0, v1, 0, 1, 2, 3, 4, 5, 6, 7);
      }
  }
  bf16x8 pg[2];
  auto expo2 = [&](f32x16& sc) {
    float ps = 0.f;
#pragma unroll
    for (int r = 0; r < 16; ++r) { sc[r] = __builtin_amdgcn_exp2f(sc[r]); ps += sc[r]; }
    l += ps;
#pragma unroll
    for (int s = 0; s < 2; ++s) {
      u32x4 w = {pk2(sc[8 * s + 0], sc[8 * s + 1]), pk2(sc[8 * s + 2], sc[8 * s + 3]), pk2(sc[8 * s + 4], sc[8 * s + 5]), pk2(sc[8 * s + 6], sc[8 * s + 7])};
      pg[s] = __builtin_bit_cast(bf16x8, w);
    }
  };
  {
    const float mx = rowmax(scur);
    const float mold = m;
    fixmax(scur, mx, mScur);
    if (__builtin_expect(__builtin_amdgcn_ballot_w64(m != mold) != 0, 0)) {
      const float alpha = __builtin_amdgcn_exp2f((mold - m) * CEXP);
#pragma unroll
      for (int s = 0; s < 2; ++s) {
        u32x4 w = __builtin_bit_cast(u32x4, pf[s]);
#pragma unroll
        for (int j = 0; j < 4; ++j) w[j] = pk2(bflo(w[j]) * alpha, bfhi(w[j]) * alpha);
        pf[s] = __builtin_bit_cast(bf16x8, w);
      }
    }
    SB();
    pv();
    expo2(scur);
#pragma unroll
    for (int i = 0; i < 8; ++i) {
      __builtin_amdgcn_sched_group_barrier(0x8, 1, 2);
      __builtin_amdgcn_sched_group_barrier(0x402, 5, 2);
    }
  }
  SB();
  const int pw = wid ^ 4;
  char* xb = smem + wid * 4096;
  float* stats = (float*)(smem + LDS_Q);
#pragma unroll
  for (int s = 0; s < 2; ++s) { o[2] = __builtin_amdgcn_mfma_f32_32x32x16_bf16(vg[2][s], pg[s], o[2], 0, 0, 0); o[3] = __builtin_amdgcn_mfma_f32_32x32x16_bf16(vg[3][s], pg[s], o[3], 0, 0, 0); }
#pragma unroll
  for (int s = 0; s < 2; ++s) { o[0] = __builtin_amdgcn_mfma_f32_32x32x16_bf16(vg[0][s], pg[s], o[0], 0, 0, 0); o[1] = __builtin_amdgcn_mfma_f32_32x32x16_bf16(vg[1][s], pg[s], o[1], 0, 0, 0); }
  {
    auto rr = __builtin_amdgcn_permlane32_swap(__float_as_uint(l), __float_as_uint(l), false, false);
    l = __uint_as_float(rr[0]) + __uint_as_float(rr[1]);
  }
  m *= CEXP;
  if (hi == 0) { stats[wid * 64 + r32] = m; stats[wid * 64 + 32 + r32] = l; }
#pragma unroll
  for (int j = 0; j < 2; ++j) {
    const f32x16& src = o[2 + j];
#pragma unroll
    for (int pr = 0; pr < 2; ++pr) {
      u32x4 w = {pk2(src[8 * pr + 0], src[8 * pr + 1]), pk2(src[8 * pr + 2], src[8 * pr + 3]), pk2(src[8 * pr + 4], src[8 * pr + 5]), pk2(src[8 * pr + 6], src[8 * pr + 7])};
      *(u32x4*)(xb + (j * 2 + pr) * 1024 + lane * 16) = w;
    }
  }
  __syncthreads();
  const float m2 = stats[pw * 64 + r32], l2 = stats[pw * 64 + 32 + r32];
  const char* pb = smem + pw * 4096;
  u32x4 rc[2][2];
#pragma unroll
  for (int j = 0; j < 2; ++j)
#pragma unroll
    for (int pr = 0; pr < 2; ++pr) rc[j][pr] = *(const u32x4*)(pb + (j * 2 + pr) * 1024 + lane * 16);
  const float M = fmaxf(m, m2);
  const float e1 = __builtin_amdgcn_exp2f(m - M), e2 = __builtin_amdgcn_exp2f(m2 - M);
  const float L = l * e1 + l2 * e2;
  const float rL = __builtin_amdgcn_rcpf(L);
  const float f1 = e1 * rL, f2 = e2 * rL;
  __amdgpu_buffer_rsrc_t rW = __builtin_amdgcn_make_buffer_rsrc((void*)ws, 0, WS_END, 0x00020000);
  const bool own_head = (g == split);
#pragma unroll
  for (int j = 0; j < 2; ++j) {
    const f32x16& own = o[j];
#pragma unroll
    for (int pr = 0; pr < 2; ++pr) {
      const u32x4 a = rc[j][pr];
      u32x4 w = {pk2(own[8 * pr + 0] * f1 + bflo(a[0]) * f2, own[8 * pr + 1] * f1 + bfhi(a[0]) * f2), pk2(own[8 * pr + 2] * f1 + bflo(a[1]) * f2, own[8 * pr + 3] * f1 + bfhi(a[1]) * f2),
                 pk2(own[8 * pr + 4] * f1 + bflo(a[2]) * f2, own[8 * pr + 5] * f1 + bfhi(a[2]) * f2), pk2(own[8 * pr + 6] * f1 + bflo(a[3]) * f2, own[8 * pr + 7] * f1 + bfhi(a[3]) * f2)};
      const int db = 2 * kh + j;
      if (own_head) *(u32x4*)(smem + LDS_OWN + (db * 2 + pr) * 1024 + lane * 16) = w;
      else __builtin_amdgcn_raw_buffer_store_b128(w, rW, WS_PO + (unsigned)((bid * 4 + g) * 8192 + (db * 2 + pr) * 1024 + lane * 16), 0, 16);
    }
  }
  {
    const unsigned long long tag = (gqa_dispatch_id() + 1ull) * 0x9E3779B97F4A7C15ull ^ 0x5DEECE66DA3C1F27ull;
    if (own_head) {
      if (kh == 0) {
        if (hi == 0) { float* st = (float*)(smem + LDS_OWN + 8192); st[r32] = M; st[32 + r32] = L; }
        if (lane < 6) {
          const int src_ = ((split + 1 + (lane >> 1)) & 3) * 2 + (lane & 1);
          unsigned long long* fin = (unsigned long long*)(ws + WS_FL + ((grp * 4 + split) * 8 + src_) * 64);
          unsigned spins = 0;
          while (__hip_atomic_load(fin, __ATOMIC_RELAXED, __HIP_MEMORY_SCOPE_AGENT) != tag) {
            __builtin_amdgcn_s_sleep(1);
            if (++spins > (1u << 18)) break;
          }
          __hip_atomic_store(fin, 0ull, __ATOMIC_RELAXED, __HIP_MEMORY_SCOPE_AGENT);
        }
      }
    } else {
      if (kh == 0 && hi == 0) {
        __builtin_amdgcn_raw_buffer_store_b32(__float_as_uint(M), rW, WS_ST + (unsigned)((bid * 4 + g) * 256 + r32 * 4), 0, 16);
        __builtin_amdgcn_raw_buffer_store_b32(__float_as_uint(L), rW, WS_ST + (unsigned)((bid * 4 + g) * 256 + 128 + r32 * 4), 0, 16);
      }
      asm volatile("s_waitcnt vmcnt(0)" ::: "memory");
      if (lane == 0) __hip_atomic_store((unsigned long long*)(ws + WS_FL + (((grp * 4 + g) * 4 + split) * 2 + kh) * 64), tag, __ATOMIC_RELAXED, __HIP_MEMORY_SCOPE_AGENT);
    }
  }
  __syncthreads();
  {
    u32x4 p[4]; float ms[4], ls[4];
#pragma unroll
    for (int s = 0; s < 4; ++s) {
      const int sp_ = (split + s) & 3;
      if (s == 0) {
        p[0] = *(const u32x4*)(smem + LDS_OWN + wid * 1024 + lane * 16);
        const float* st = (const float*)(smem + LDS_OWN + 8192);
        ms[0] = st[r32]; ls[0] = st[32 + r32];
      } else {
        const unsigned u = (unsigned)((grp * 4 + sp_) * 4 + split);
        p[s] = __builtin_amdgcn_raw_buffer_load_b128(rW, WS_PO + u * 8192 + wid * 1024 + lane * 16, 0, 16);
        ms[s] = __uint_as_float(__builtin_amdgcn_raw_buffer_load_b32(rW, WS_ST + u * 256 + r32 * 4, 0, 16));
        ls[s] = __uint_as_float(__builtin_amdgcn_raw_buffer_load_b32(rW, WS_ST + u * 256 + 128 + r32 * 4, 0, 16));
      }
    }
    const float Mx = fmaxf(fmaxf(ms[0], ms[1]), fmaxf(ms[2], ms[3]));
    float wt[4], W = 0.f;
#pragma unroll
    for (int s = 0; s < 4; ++s) { wt[s] = ls[s] * __builtin_amdgcn_exp2f(ms[s] - Mx); W += wt[s]; }
    const float rWt = __builtin_amdgcn_rcpf(W);
    float acc[8];
#pragma unroll
    for (int j = 0; j < 8; ++j) acc[j] = 0.f;
#pragma unroll
    for (int s = 0; s < 4; ++s) {
      const float ww = wt[s] * rWt;
#pragma unroll
      for (int j = 0; j < 4; ++j) { acc[2 * j] += ww * bflo(p[s][j]); acc[2 * j + 1] += ww * bfhi(p[s][j]); }
    }
    const int db = wid >> 1, pr = wid & 1;
    __hip_bfloat16* orow = Op + ((size_t)(grp * 4 + split) * SQ + r32) * D + db * 32 + pr * 16 + 4 * hi;
    u32x2 a = {pk2(acc[0], acc[1]), pk2(acc[2], acc[3])};
    u32x2 b = {pk2(acc[4], acc[5]), pk2(acc[6], acc[7])};
    *(u32x2*)(orow) = a;
    *(u32x2*)(orow + 8) = b;
  }
}

__global__ __launch_bounds__(64) void gqa_code_end() {}

extern "C" void kernel_launch(void* const* d_in, const int* in_sizes, int n_in, void* d_out, int out_size, void* d_ws, size_t ws_size,
                              hipStream_t stream) {
  const __hip_bfloat16* q = (const __hip_bfloat16*)d_in[0];
  const __hip_bfloat16* k = (const __hip_bfloat16*)d_in[1];
  const __hip_bfloat16* v = (const __hip_bfloat16*)d_in[2];
  __hip_bfloat16* o = (__hip_bfloat16*)d_out;
  hipLaunchKernelGGL(gqa_split_kernel, dim3(256), dim3(512), 0, stream, q, k, v, (char*)d_ws, o);
}
```
